```python
import torch

def _softmax(x, axis=-1):
    return torch.softmax(x, dim=axis)
def _gelu(x):
    return torch.nn.functional.gelu(x, approximate="tanh")
def _silu(x):
    return torch.nn.functional.silu(x)
def _sigmoid(x):
    return torch.sigmoid(x)
def _layernorm(x, axis=-1, eps=1e-5):
    m = x.mean(dim=axis, keepdim=True)
    v = x.var(dim=axis, keepdim=True, unbiased=False)
    return (x - m) / torch.sqrt(v + eps)
def _scatter_add(tgt, idx, src, axis=0):
    return tgt.clone().index_add_(axis, idx, src)
def _segment_sum(src, seg, n):
    out = torch.zeros((n,) + src.shape[1:], dtype=src.dtype)
    return out.index_add_(0, seg, src)
def _pad(x, pw):
    flat = tuple(v for pair in reversed(pw) for v in pair)
    return torch.nn.functional.pad(x, flat)

kernel_name = 'sequential_preLN__softmax_full_mha__geglu_ffn__prompt'

def setup_inputs():

    _w = (lambda a: (lambda t: t.to(torch.bfloat16) if t.is_floating_point() else t)(torch.as_tensor(a)))
    def _A_setup():
        B, S, D = 1, 2048, 576
        return {
            "x": _w((lambda *s: torch.randn(*s, dtype=torch.bfloat16) * 0.05)(B, S, D)),
            "wqkv": _w((lambda *s: torch.randn(*s, dtype=torch.bfloat16) * 0.05)(D, 3 * D)),
            "wo": _w((lambda *s: torch.randn(*s, dtype=torch.bfloat16) * 0.05)(D, D)),
        }
    _A_in = _A_setup()

    def _B_setup():
        B, S, D, F = 1, 2048, 576, 4608
        return {
            "x": _w((lambda *s: torch.randn(*s, dtype=torch.bfloat16) * 0.05)(B, S, D)),
            "w_gate": _w((lambda *s: torch.randn(*s, dtype=torch.bfloat16) * 0.05)(D, F)),
            "w_up": _w((lambda *s: torch.randn(*s, dtype=torch.bfloat16) * 0.05)(D, F)),
            "w_down": _w((lambda *s: torch.randn(*s, dtype=torch.bfloat16) * 0.05)(F, D)),
        }
    _B_in = _B_setup()

    return {
        "A_x": _A_in["x"],
        "A_wqkv": _A_in["wqkv"],
        "A_wo": _A_in["wo"],
        "B_x": _B_in["x"],
        "B_w_gate": _B_in["w_gate"],
        "B_w_up": _B_in["w_up"],
        "B_w_down": _B_in["w_down"],
    }

def reference(A_x, A_wqkv, A_wo, B_x, B_w_gate, B_w_up, B_w_down):
    def _A_ref(x, wqkv, wo):
        B, S, D = x.shape
        H = 32; Dh = D // H
        qkv = (lambda x, *d: x.permute(*d))((x @ wqkv).reshape(B, S, 3, H, Dh), 2, 0, 3, 1, 4)
        q, k, v = qkv[0], qkv[1], qkv[2]
        scale = Dh ** -0.5
        scores = torch.einsum("bhqd,bhkd->bhqk", q, k) * scale
        p = _softmax(scores, axis=-1)
        o = torch.einsum("bhqk,bhkd->bhqd", p, v)
        return (lambda x, *d: x.permute(*d))(o, 0, 2, 1, 3).reshape(B, S, D) @ wo

    def _B_ref(x, w_gate, w_up, w_down):
        return (_gelu(x @ w_gate) * (x @ w_up)) @ w_down

    x = A_x
    h = x + _A_ref(_layernorm(x, axis=-1), A_wqkv, A_wo)
    h2 = h + _B_ref(_layernorm(h, axis=-1), B_w_gate, B_w_up, B_w_down)
    return h2
```

```cpp
#include <hip/hip_runtime.h>
#include <stdint.h>

typedef unsigned short u16;
typedef __attribute__((ext_vector_type(8))) __bf16 bf16x8;
typedef __attribute__((ext_vector_type(2))) __bf16 bf16x2;
typedef __attribute__((ext_vector_type(4))) short s16x4;
typedef __attribute__((ext_vector_type(16))) float f32x16;
typedef __attribute__((ext_vector_type(4))) float f32x4;
typedef __attribute__((ext_vector_type(2))) float f32x2;
typedef __attribute__((ext_vector_type(4))) unsigned u32x4;
typedef __attribute__((ext_vector_type(2))) unsigned u32x2;
#define LAS __attribute__((address_space(3)))
typedef LAS char* lptr;

constexpr int S_ = 2048, D_ = 576, F_ = 4608;
constexpr int QTILE = 1152, KVTILE = 2496;
constexpr size_t OFF_QIMG = 0;
constexpr size_t OFF_KV   = OFF_QIMG + (size_t)32 * 64 * QTILE;
constexpr size_t OFF_OT   = OFF_KV + (size_t)32 * 64 * KVTILE;
constexpr size_t OFF_HT   = OFF_OT + (size_t)64 * 36 * 1024;
constexpr size_t OFF_STAT = OFF_HT + (size_t)64 * 36 * 1024;
constexpr size_t OFF_WOT  = OFF_STAT + (size_t)4 * 2048 * 8;
constexpr size_t OFF_WGU  = OFF_WOT + (size_t)18 * 36 * 1024;
constexpr size_t OFF_CSP  = OFF_WGU + (size_t)288 * 36 * 1024;
constexpr size_t OFF_WDT  = OFF_CSP + (size_t)9 * 288 * 32 * 4;
constexpr size_t OFF_ACT  = OFF_WDT + (size_t)18 * 288 * 1024;
constexpr size_t OFF_PART = OFF_ACT + (size_t)64 * 288 * 1024;
constexpr size_t OFF_END  = OFF_PART + (size_t)240 * 98304;

__device__ __forceinline__ f32x16 mfma32(u32x4 a, u32x4 b, f32x16 c) {
    return __builtin_amdgcn_mfma_f32_32x32x16_bf16(__builtin_bit_cast(bf16x8, a), __builtin_bit_cast(bf16x8, b), c, 0, 0, 0);
}
__device__ __forceinline__ f32x16 mfma32k8(u32x2 a, u32x2 b, f32x16 c) {
    return __builtin_amdgcn_mfma_f32_32x32x8bf16_1k(__builtin_bit_cast(s16x4, a), __builtin_bit_cast(s16x4, b), c, 0, 0, 0);
}
__device__ __forceinline__ unsigned pk2(float lo, float hi) {
    f32x2 v = {lo, hi};
    return __builtin_bit_cast(unsigned, __builtin_convertvector(v, bf16x2));
}
__device__ __forceinline__ float bflo(unsigned u) { return __uint_as_float(u << 16); }
__device__ __forceinline__ float bfhi(unsigned u) { return __uint_as_float(u & 0xffff0000u); }
__device__ __forceinline__ void dma16(unsigned voff, const void* sbase, unsigned lds) {
    unsigned sv;
    asm volatile("s_mov_b32 %0, m0\n\ts_mov_b32 m0, %3\n\ts_nop 0\n\tglobal_load_lds_dwordx4 %1, %2\n\ts_mov_b32 m0, %0"
                 : "=&s"(sv) : "v"(voff), "s"(sbase), "s"(lds) : "memory");
}
#define WAIT_VM(n) asm volatile("s_waitcnt vmcnt(" #n ")" ::: "memory")
#define WAIT_VM_BAR(n) asm volatile("s_waitcnt vmcnt(" #n ") lgkmcnt(0)\n\ts_barrier" ::: "memory")
#define SBAR() __builtin_amdgcn_sched_barrier(0)
__device__ __forceinline__ u32x4 lds128(lptr p) { return *(LAS const u32x4*)p; }
__device__ __forceinline__ u32x2 lds64(lptr p) { return *(LAS const u32x2*)p; }
typedef short v4i16_t __attribute__((ext_vector_type(4)));
__device__ __forceinline__ u32x2 ldstr(lptr p) { return __builtin_bit_cast(u32x2, __builtin_amdgcn_ds_read_tr16_b64_v4i16((LAS v4i16_t*)p)); }
__device__ __forceinline__ float xhalf(float v) {
    return __shfl_xor(v, 32);
}
__device__ __forceinline__ int crow(int r, int hh) { return (r & 3) + 8 * (r >> 2) + 4 * hh; }

constexpr int K1_BUF = 32768, K1_NB = 4, K1_ST = K1_NB * K1_BUF;
constexpr float QSCALE = 0.23570226039551584f * 1.4426950408889634f;

template <bool V0, bool V1>
__device__ __forceinline__ void k1_body(const u16* __restrict__ x, const u16* __restrict__ w, char* __restrict__ ws, lptr lds,
                                        int mt, int g, int wid, int lane) {
    const int a = wid >> 1, b = wid & 1;
    const int hh = lane >> 5, l31 = lane & 31;
    const int t0 = 2 * b + a, t1 = 2 * b + (1 - a);
    const int f0 = 2 * a + b, f1 = 2 * a + (1 - b);
    const unsigned ldsbase = (unsigned)(uintptr_t)lds;
    unsigned xoff[4], woff[4];
#pragma unroll
    for (int i = 0; i < 4; ++i) {
        const int p = 4 * wid + i;
        const int row = 8 * p + (lane >> 3), ch = (lane & 7) ^ ((row >> 1) & 7);
        xoff[i] = row * 1152 + ch * 16;
        const int kr = 4 * p + (lane >> 4), c = (lane & 15) ^ ((kr & 3) << 2);
        const int cs = c & 3;
        const int byte = c < 12 ? 72 * (c >> 2) + (cs < 2 ? 16 * cs : 40 + 16 * (cs - 2)) : 72 * ((c < 14 ? c : 14) - 12) + 32;
        woff[i] = kr * 3456 + byte;
    }
    const char* xb = (const char*)x + (size_t)mt * 128 * 1152;
    const char* wb = (const char*)w + g * 216;
    auto issue = [&](int s) {
        const unsigned dst = ldsbase + (s & 3) * K1_BUF + wid * 4096;
#pragma unroll
        for (int i = 0; i < 4; ++i) dma16(xoff[i], xb + s * 128, dst + i * 1024);
#pragma unroll
        for (int i = 0; i < 4; ++i) dma16(woff[i], wb + (size_t)s * 64 * 3456, dst + 16384 + i * 1024);
    };
    const int sw = (l31 >> 1) & 7;
    const int xa0 = (32 * t0 + l31) * 128, xa1 = (32 * t1 + l31) * 128;
    const int xc = (hh ^ sw) << 4;
    const int q4 = (lane >> 2) & 3, p4 = lane & 3, cg = (lane >> 4) & 1;
    auto waddr = [&](int f) {
        const int c = f < 3 ? 4 * f + 2 * cg + (p4 >> 1) : 12 + p4;
        const int sub = f < 3 ? 8 * (p4 & 1) : 0;
        return 16384 + (8 * hh + q4) * 256 + ((c ^ (q4 << 2)) << 4) + sub;
    };
    const int wa0 = waddr(f0), wa1 = waddr(f1);

    f32x16 acc00 = {}, acc01 = {}, acc10 = {}, acc11 = {};
    f32x2 s1 = {0.f, 0.f}, s2 = {0.f, 0.f}, c1 = {0.f, 0.f};
    issue(0); issue(1); issue(2);
    for (int s = 0; s < 9; ++s) {
        if (s < 7) WAIT_VM_BAR(16); else if (s == 7) WAIT_VM_BAR(8); else WAIT_VM_BAR(0);
        if (s < 6) issue(s + 3);
        lptr buf = lds + (s & 3) * K1_BUF;
#pragma unroll
        for (int ks = 0; ks < 4; ++ks) {
            const u32x4 x0 = lds128(buf + xa0 + (xc ^ (ks << 5)));
            const u32x4 x1 = lds128(buf + xa1 + (xc ^ (ks << 5)));
            const u32x2 a0 = ldstr(buf + wa0 + ks * 4096), a1 = ldstr(buf + wa0 + ks * 4096 + 1024);
            const u32x2 b0 = ldstr(buf + wa1 + ks * 4096), b1 = ldstr(buf + wa1 + ks * 4096 + 1024);
            const u32x4 w0 = {a0[0], a0[1], a1[0], a1[1]}, w1 = {b0[0], b0[1], b1[0], b1[1]};
            if (V0) { acc00 = mfma32(x0, w0, acc00); acc01 = mfma32(x1, w0, acc01); }
            else    { acc00 = mfma32(w0, x0, acc00); acc01 = mfma32(w0, x1, acc01); }
            if (V1) { acc10 = mfma32(x0, w1, acc10); acc11 = mfma32(x1, w1, acc11); }
            else    { acc10 = mfma32(w1, x0, acc10); acc11 = mfma32(w1, x1, acc11); }
#pragma unroll
            for (int i = 0; i < 4; ++i) {
                const f32x2 xv = {bflo(x0[i]), bfhi(x0[i])};
                s1 += xv; s2 += xv * xv;
                const f32x2 wv = {bflo(w0[i]), bfhi(w0[i])};
                c1 += wv;
            }
        }
        SBAR();
    }
    LAS float* st = (LAS float*)(lds + K1_ST);
    LAS float* cs = (LAS float*)(lds + K1_ST + 1024);
    {
        float a1 = s1[0] + s1[1], a2 = s2[0] + s2[1], c = c1[0] + c1[1];
        a1 += xhalf(a1); a2 += xhalf(a2); c += xhalf(c);
        const float mean = a1 * (1.f / 576.f);
        const float var = fmaxf(a2 * (1.f / 576.f) - mean * mean, 0.f);
        const float rstd = 1.0f / sqrtf(var + 1e-5f);
        if (hh == 0) { st[(32 * t0 + l31) * 2] = rstd; st[(32 * t0 + l31) * 2 + 1] = mean * rstd; cs[32 * f0 + l31] = c; }
    }
    __syncthreads();
    char* qimg = ws + OFF_QIMG; char* kv = ws + OFF_KV;
    auto finish = [&](const f32x16& acc, int f, int t, bool isV) {
        const int tt = 4 * mt + t;
        float v[16];
        if (f < 3 && isV) {
            const int P = 3 * g + f, pp = P & 15;
            const float c = cs[32 * f + l31];
#pragma unroll
            for (int r = 0; r < 16; ++r) {
                const f32x2 rm = *(LAS const f32x2*)(st + (32 * t + crow(r, hh)) * 2);
                v[r] = acc[r] * rm[0] - rm[1] * c;
            }
            const int h = 2 * pp + (l31 >> 4), d = l31 & 15;
            char* dst = kv + (size_t)(h * 64 + tt) * KVTILE + 1280 + hh * 304 + d * 16;
            *(u32x4*)dst = (u32x4){pk2(v[0], v[1]), pk2(v[2], v[3]), pk2(v[4], v[5]), pk2(v[6], v[7])};
            *(u32x4*)(dst + 608) = (u32x4){pk2(v[8], v[9]), pk2(v[10], v[11]), pk2(v[12], v[13]), pk2(v[14], v[15])};
            return;
        }
        const f32x2 rm = *(LAS const f32x2*)(st + (32 * t + l31) * 2);
#pragma unroll
        for (int r = 0; r < 16; ++r) v[r] = acc[r] * rm[0] - rm[1] * cs[32 * f + crow(r, hh)];
        if (f < 3) {
            const int P = 3 * g + f, sec = P >> 4, pp = P & 15;
            const float sc = sec == 0 ? QSCALE : 1.f;
            char* base = sec == 0 ? qimg + (size_t)(2 * pp * 64 + tt) * QTILE : kv + (size_t)(2 * pp * 64 + tt) * KVTILE;
            const size_t hs = sec == 0 ? (size_t)64 * QTILE : (size_t)64 * KVTILE;
            *(u32x4*)(base + lane * 16) = (u32x4){pk2(v[0] * sc, v[1] * sc), pk2(v[2] * sc, v[3] * sc), pk2(v[4] * sc, v[5] * sc), pk2(v[6] * sc, v[7] * sc)};
            *(u32x4*)(base + hs + lane * 16) = (u32x4){pk2(v[8] * sc, v[9] * sc), pk2(v[10] * sc, v[11] * sc), pk2(v[12] * sc, v[13] * sc), pk2(v[14] * sc, v[15] * sc)};
        } else {
#pragma unroll
            for (int u = 0; u < 2; ++u) {
                const int p = 2 * u + hh;
                if (p < 3) {
                    const int P = 3 * g + p, sec = P >> 4, pp = P & 15;
#pragma unroll
                    for (int o = 0; o < 2; ++o) {
                        const int h = 2 * pp + o;
                        const float e0 = v[4 * u + 2 * o], e1 = v[4 * u + 2 * o + 1];
                        if (sec == 0) {
                            *(unsigned*)(qimg + (size_t)(h * 64 + tt) * QTILE + 1024 + l31 * 4) = pk2(e0 * QSCALE, e1 * QSCALE);
                        } else if (sec == 1) {
                            const unsigned pk = pk2(e0, e1);
                            *(u32x2*)(kv + (size_t)(h * 64 + tt) * KVTILE + 1024 + l31 * 8) = (u32x2){pk, pk};
                        } else {
                            const unsigned pk = pk2(e0, e1);
                            const int off = (2 * (l31 >> 4) + ((l31 >> 2) & 1)) * 304 + (((l31 >> 3) & 1) * 4 + (l31 & 3)) * 2;
                            char* dst = kv + (size_t)(h * 64 + tt) * KVTILE + 1280 + off;
                            *(u16*)(dst + 16 * 16) = (u16)(pk & 0xffff);
                            *(u16*)(dst + 17 * 16) = (u16)(pk >> 16);
                            *(u16*)(dst + 18 * 16) = (u16)0x3F80;
                        }
                    }
                }
            }
        }
    };
    finish(acc00, f0, t0, V0); finish(acc01, f0, t1, V0); finish(acc10, f1, t0, V1); finish(acc11, f1, t1, V1);
}

__global__ __launch_bounds__(256, 1) void k1_qkv(const u16* __restrict__ x, const u16* __restrict__ w, char* __restrict__ ws) {
    __shared__ __attribute__((aligned(16))) char smem[K1_ST + 2048];
    const int lane = threadIdx.x & 63, wid = __builtin_amdgcn_readfirstlane(threadIdx.x >> 6);
    const int bx = blockIdx.x, xcd = bx & 7, j = bx >> 3;
    const int mt = (xcd >> 1) * 4 + (j & 3), g = (xcd & 1) * 8 + (j >> 2);
    const int a = wid >> 1, b = wid & 1;
    const int f0 = 2 * a + b, f1 = 2 * a + (1 - b);
    const bool v0 = f0 < 3 && (3 * g + f0) >= 32, v1 = f1 < 3 && (3 * g + f1) >= 32;
    lptr lds = (lptr)smem;
    if (v0) { if (v1) k1_body<true, true>(x, w, ws, lds, mt, g, wid, lane); else k1_body<true, false>(x, w, ws, lds, mt, g, wid, lane); }
    else    { if (v1) k1_body<false, true>(x, w, ws, lds, mt, g, wid, lane); else k1_body<false, false>(x, w, ws, lds, mt, g, wid, lane); }
}

__device__ __forceinline__ int phi32(int rho) { const int r = (rho & 3) + 4 * (rho >> 3), hc = (rho >> 2) & 1; return 16 * (r >> 3) + 8 * hc + (r & 7); }
__device__ __forceinline__ int pi16(int c) { return 8 * ((c >> 2) & 1) + 4 * (c >> 3) + (c & 3); }

__device__ __forceinline__ u32x4 gather8(const u16* p, const int (&koff)[8]) {
    unsigned e[8];
#pragma unroll
    for (int j = 0; j < 8; ++j) e[j] = p[koff[j]];
    return (u32x4){e[0] | (e[1] << 16), e[2] | (e[3] << 16), e[4] | (e[5] << 16), e[6] | (e[7] << 16)};
}
__device__ __forceinline__ void pretile_item(int item, int lane, const u16* wo, const u16* wg, const u16* wu, const u16* wd, char* ws) {
    const int rho = lane & 31, hh = lane >> 5;
    if (item < 2592) {
        const int ft = item / 9, kq = item % 9;
        const u16* src = (rho < 16 ? wg : wu) + 16 * ft + pi16(rho & 15);
        float cs = 0.f;
#pragma unroll
        for (int i = 0; i < 4; ++i) {
            const int ks = 4 * kq + i;
            int koff[8];
#pragma unroll
            for (int j = 0; j < 8; ++j) koff[j] = (16 * ks + 8 * hh + j) * F_;
            const u32x4 v = gather8(src, koff);
            *(u32x4*)(ws + OFF_WGU + (size_t)(ft * 36 + ks) * 1024 + lane * 16) = v;
#pragma unroll
            for (int j = 0; j < 4; ++j) cs += bflo(v[j]) + bfhi(v[j]);
        }
        cs += xhalf(cs);
        if (hh == 0) *(float*)(ws + OFF_CSP + (size_t)((kq * 288 + ft) * 32 + rho) * 4) = cs;
    } else if (item < 3888) {
        const int it = item - 2592, dT = it / 72, kq = it % 72;
        const u16* src = wd + 32 * dT + phi32(rho);
#pragma unroll
        for (int i = 0; i < 4; ++i) {
            const int ks = 4 * kq + i;
            int koff[8];
#pragma unroll
            for (int j = 0; j < 8; ++j) koff[j] = (16 * ks + 8 * hh + j) * D_;
            *(u32x4*)(ws + OFF_WDT + (size_t)(dT * 288 + ks) * 1024 + lane * 16) = gather8(src, koff);
        }
    } else if (item < 4050) {
        const int it = item - 3888, dT = it / 9, kq = it % 9;
        const u16* src = wo + 32 * dT + phi32(rho);
#pragma unroll
        for (int i = 0; i < 4; ++i) {
            const int ks = 4 * kq + i;
            int koff[8];
#pragma unroll
            for (int j = 0; j < 8; ++j) {
                int row;
                if (ks < 32) { const int h = ks, md = 8 * (j >> 2) + 4 * hh + (j & 3); row = 18 * h + md + ((h & 1) ? 2 : 0); }
                else { const int p = 8 * hh + j, h = 8 * (ks - 32) + (p >> 1), e = p & 1; row = 18 * h + ((h & 1) ? e : 16 + e); }
                koff[j] = row * D_;
            }
            *(u32x4*)(ws + OFF_WOT + (size_t)(dT * 36 + ks) * 1024 + lane * 16) = gather8(src, koff);
        }
    }
}

constexpr int K2_LDS = 64 * KVTILE;

struct AttnFrag { u32x4 km; u32x2 kt; };

__global__ __launch_bounds__(512, 2) void k2_attn(const u16* __restrict__ wo, const u16* __restrict__ wg, const u16* __restrict__ wu,
                                                  const u16* __restrict__ wd, char* __restrict__ ws) {
    __shared__ __attribute__((aligned(16))) char smem[K2_LDS];
    const int lane = threadIdx.x & 63, wid = __builtin_amdgcn_readfirstlane(threadIdx.x >> 6);
    const int bx = blockIdx.x, xcd = bx & 7, j = bx >> 3;
    const int h = 4 * xcd + (j >> 3), qb = j & 7, tt = 8 * qb + wid;
    const int hh = lane >> 5, l31 = lane & 31;
    lptr lds = (lptr)smem;
    const unsigned ldsbase = (unsigned)(uintptr_t)lds;
    u32x4 qm; unsigned qt;
    {
        const char* qsrc = ws + OFF_QIMG + (size_t)(h * 64 + tt) * QTILE;
        const unsigned o1 = lane * 16, o2 = 1024 + l31 * 4;
        asm volatile("global_load_dwordx4 %0, %2, %4\n\tglobal_load_dword %1, %3, %4" : "=&v"(qm), "=&v"(qt) : "v"(o1), "v"(o2), "s"(qsrc) : "memory");
    }
    const char* kvsrc = ws + OFF_KV + (size_t)h * K2_LDS;
#pragma unroll
    for (int i = 0; i < 20; ++i) {
        int p = wid + 8 * i; p = p < 156 ? p : 155;
        dma16(lane * 16, kvsrc + p * 1024, ldsbase + p * 1024);
    }
    const lptr kp = lds + lane * 16;
    const lptr ktp = lds + 1024 + l31 * 8;
    const lptr vp = lds + 1280 + hh * 304 + (l31 < 18 ? l31 : 18) * 16;
    asm volatile("s_waitcnt vmcnt(18)\n\ts_barrier" : "+v"(qm), "+v"(qt) :: "memory");
    const u32x2 qtf = {hh == 0 ? qt : 0u, 0u};
    f32x16 o = {};
    const f32x16 zero = {};
#define LDK(t, KM, KT) do { KM = lds128(kp + (t) * KVTILE); KT = lds64(ktp + (t) * KVTILE); } while (0)
#define LDV(t, V0, V1) do { V0 = lds128(vp + (t) * KVTILE); V1 = lds128(vp + (t) * KVTILE + 608); } while (0)
#define QK(KM, KT) mfma32(KM, qm, mfma32k8(KT, qtf, zero))
    u32x4 km, v0, v1; u32x2 kt;
    LDK(0, km, kt);
    f32x16 sA = QK(km, kt), sB;
    LDK(1, km, kt); LDV(0, v0, v1);
#define STEP(t, SC, SN, LAST) do { \
        if (!(LAST)) { SN = QK(km, kt); } \
        if ((t) + 2 < 64) LDK((t) + 2, km, kt); \
        unsigned pw[8]; \
        _Pragma("unroll") for (int i = 0; i < 8; ++i) pw[i] = pk2(__builtin_amdgcn_exp2f(SC[2 * i]), __builtin_amdgcn_exp2f(SC[2 * i + 1])); \
        o = mfma32(v0, (u32x4){pw[0], pw[1], pw[2], pw[3]}, o); \
        o = mfma32(v1, (u32x4){pw[4], pw[5], pw[6], pw[7]}, o); \
        if (!(LAST)) LDV((t) + 1, v0, v1); \
    } while (0)
    int t = 0;
    for (; t < 4; t += 2) { STEP(t, sA, sB, false); STEP(t + 1, sB, sA, false); }
    asm volatile("s_waitcnt vmcnt(10)\n\ts_barrier" ::: "memory");
    for (; t < 16; t += 2) { STEP(t, sA, sB, false); STEP(t + 1, sB, sA, false); }
    asm volatile("s_waitcnt vmcnt(0)\n\ts_barrier" ::: "memory");
    for (; t < 62; t += 2) { STEP(t, sA, sB, false); STEP(t + 1, sB, sA, false); }
    STEP(62, sA, sB, false); STEP(63, sB, sA, true);
    float l = __shfl(o[10], l31);
    const bool bad = !(l > 8.673617379884035e-19f && l < 1.152921504606847e18f);
    if (__builtin_expect(__any(bad), 0)) {
        o = zero; float m = -3.0e38f;
        for (int tb = 0; tb < 64; ++tb) {
            LDK(tb, km, kt); LDV(tb, v0, v1);
            f32x16 s = QK(km, kt);
            float mx = s[0];
#pragma unroll
            for (int r = 1; r < 16; ++r) mx = fmaxf(mx, s[r]);
            mx = fmaxf(mx, xhalf(mx));
            const float mn = fmaxf(m, mx), al = __builtin_amdgcn_exp2f(m - mn);
            m = mn;
            unsigned pw[8];
#pragma unroll
            for (int i = 0; i < 8; ++i) pw[i] = pk2(__builtin_amdgcn_exp2f(s[2 * i] - mn), __builtin_amdgcn_exp2f(s[2 * i + 1] - mn));
#pragma unroll
            for (int r = 0; r < 16; ++r) o[r] *= al;
            o = mfma32(v0, (u32x4){pw[0], pw[1], pw[2], pw[3]}, o);
            o = mfma32(v1, (u32x4){pw[4], pw[5], pw[6], pw[7]}, o);
        }
        l = __shfl(o[10], l31);
    }
    const float inv = 1.0f / l;
    char* ot = ws + OFF_OT + (size_t)tt * 36 * 1024;
    *(u32x4*)(ot + h * 1024 + lane * 16) = (u32x4){pk2(o[0] * inv, o[1] * inv), pk2(o[2] * inv, o[3] * inv), pk2(o[4] * inv, o[5] * inv), pk2(o[6] * inv, o[7] * inv)};
    if (hh == 0) *(unsigned*)(ot + (32 + (h >> 3)) * 1024 + (((h & 7) >> 2) * 32 + l31) * 16 + (h & 3) * 4) = pk2(o[8] * inv, o[9] * inv);
    const int gw = bx * 8 + wid;
    pretile_item(gw, lane, wo, wg, wu, wd, ws);
    pretile_item(gw + 2048, lane, wo, wg, wu, wd, ws);
#undef LDK
#undef LDV
#undef QK
#undef STEP
}

__global__ __launch_bounds__(320, 1) void k3_wo(const u16* __restrict__ x, char* __restrict__ ws) {
    __shared__ __attribute__((aligned(16))) char smem[36 * 1024 + 5 * 32 * 8];
    const int lane = threadIdx.x & 63, wid = __builtin_amdgcn_readfirstlane(threadIdx.x >> 6);
    const int bx = blockIdx.x, tt = (bx & 7) * 8 + ((bx >> 3) & 7), cg = bx >> 6;
    const int hh = lane >> 5, l31 = lane & 31;
    const int dT0 = cg < 2 ? 5 * cg : 10 + 4 * (cg - 2), nT = cg < 2 ? 5 : 4;
    lptr lds = (lptr)smem;
    const unsigned ldsbase = (unsigned)(uintptr_t)lds;
    const char* osrc = ws + OFF_OT + (size_t)tt * 36 * 1024;
#pragma unroll
    for (int i = 0; i < 8; ++i) { int p = wid + 5 * i; p = p < 36 ? p : 35; dma16(lane * 16, osrc + p * 1024, ldsbase + p * 1024); }
    const bool act = wid < nT;
    const int dT = dT0 + (act ? wid : 0);
    const u32x4* wsrc = (const u32x4*)(ws + OFF_WOT + (size_t)dT * 36 * 1024) + lane;
    u32x4 af[36];
#pragma unroll
    for (int k = 0; k < 36; ++k) af[k] = wsrc[k * 64];
    const u16* xr = x + (size_t)(32 * tt + l31) * D_ + 32 * dT + 8 * hh;
    const u32x4 xr0 = *(const u32x4*)xr, xr1 = *(const u32x4*)(xr + 16);
    asm volatile("s_waitcnt vmcnt(38)\n\ts_barrier" ::: "memory");
    f32x16 acc = {};
#pragma unroll
    for (int k = 0; k < 36; ++k) acc = mfma32(af[k], lds128(lds + k * 1024 + lane * 16), acc);
    float s1 = 0.f, s2 = 0.f;
    unsigned hp[8];
#pragma unroll
    for (int i = 0; i < 8; ++i) {
        const unsigned xv = i < 4 ? xr0[i] : xr1[i - 4];
        hp[i] = pk2(acc[2 * i] + bflo(xv), acc[2 * i + 1] + bfhi(xv));
        const float a = bflo(hp[i]), b = bfhi(hp[i]);
        s1 += a + b; s2 += a * a + b * b;
    }
    s1 += xhalf(s1); s2 += xhalf(s2);
    LAS f32x2* red = (LAS f32x2*)(lds + 36 * 1024);
    if (act) {
        char* ht = ws + OFF_HT + (size_t)(tt * 36 + 2 * dT) * 1024 + lane * 16;
        *(u32x4*)ht = (u32x4){hp[0], hp[1], hp[2], hp[3]};
        *(u32x4*)(ht + 1024) = (u32x4){hp[4], hp[5], hp[6], hp[7]};
    }
    if (hh == 0) red[wid * 32 + l31] = act ? (f32x2){s1, s2} : (f32x2){0.f, 0.f};
    __syncthreads();
    if (threadIdx.x < 32) {
        f32x2 r = red[l31];
#pragma unroll
        for (int i = 1; i < 5; ++i) r += red[i * 32 + l31];
        *(f32x2*)(ws + OFF_STAT + (size_t)(cg * 2048 + 32 * tt + l31) * 8) = r;
    }
}

constexpr int K4_SLOT = 36 * 1024, K4_CS = 4 * K4_SLOT;

__device__ __forceinline__ void k4_epi(const f32x16& acc, LAS const float* cs, float m, float C0, float C1, float V, char* dst) {
    float hv[8];
    const f32x4 g0 = *(LAS const f32x4*)(cs), g1 = *(LAS const f32x4*)(cs + 8), u0 = *(LAS const f32x4*)(cs + 16), u1 = *(LAS const f32x4*)(cs + 24);
#pragma unroll
    for (int r = 0; r < 8; ++r) {
        const float g = __builtin_fmaf(-m, r < 4 ? g0[r & 3] : g1[r & 3], acc[r]);
        const float u = __builtin_fmaf(-m, r < 4 ? u0[r & 3] : u1[r & 3], acc[r + 8]);
        const float t = g * g;
        const float e = __builtin_amdgcn_exp2f(g * __builtin_fmaf(t, C1, C0));
        const float rr = __builtin_amdgcn_rcpf(__builtin_fmaf(e, V, V));
        hv[r] = g * u * rr;
    }
    *(u32x4*)dst = (u32x4){pk2(hv[0], hv[1]), pk2(hv[2], hv[3]), pk2(hv[4], hv[5]), pk2(hv[6], hv[7])};
}

__global__ __launch_bounds__(512, 2) void k4_gateup(char* __restrict__ ws) {
    __shared__ __attribute__((aligned(16))) char smem[K4_CS + 288 * 4];
    const int tid = threadIdx.x, lane = tid & 63, wid = __builtin_amdgcn_readfirstlane(tid >> 6);
    const int bx = blockIdx.x, xcd = bx & 7, j = bx >> 3;
    const int mt = j & 7, fg = 4 * xcd + (j >> 3);
    const int tt = 8 * mt + wid, ft0 = 9 * fg;
    const int hh = lane >> 5, l31 = lane & 31;
    lptr lds = (lptr)smem;
    const unsigned ldsbase = (unsigned)(uintptr_t)lds;
    const char* wsrc = ws + OFF_WGU + (size_t)ft0 * K4_SLOT;
    auto issue = [&](int f) {
        const unsigned dst = ldsbase + (f & 3) * K4_SLOT;
#pragma unroll
        for (int i = 0; i < 4; ++i) dma16(lane * 16, wsrc + (size_t)f * K4_SLOT + (wid + 8 * i) * 1024, dst + (wid + 8 * i) * 1024);
        if (wid < 4) dma16(lane * 16, wsrc + (size_t)f * K4_SLOT + (wid + 32) * 1024, dst + (wid + 32) * 1024);
    };
    float csp[9];
    {
        const int c = tid < 288 ? tid : 0;
        const float* p = (const float*)(ws + OFF_CSP) + (size_t)ft0 * 32 + c;
#pragma unroll
        for (int i = 0; i < 9; ++i) csp[i] = p[(size_t)i * 288 * 32];
    }
    f32x2 stp[4];
#pragma unroll
    for (int i = 0; i < 4; ++i) stp[i] = *(const f32x2*)(ws + OFF_STAT + (size_t)(i * 2048 + 32 * tt + l31) * 8);
    issue(0);
    u32x4 xf[36];
    {
        const u32x4* hsrc = (const u32x4*)(ws + OFF_HT + (size_t)tt * 36 * 1024) + lane;
#pragma unroll
        for (int k = 0; k < 36; ++k) xf[k] = hsrc[k * 64];
    }
    issue(1); issue(2);
    LAS float* csl = (LAS float*)(lds + K4_CS);
    {
        float c = 0.f;
#pragma unroll
        for (int i = 0; i < 9; ++i) c += csp[i];
        if (tid < 288) csl[tid] = c;
    }
    const f32x2 st = stp[0] + stp[1] + stp[2] + stp[3];
    const float mean = st[0] * (1.f / 576.f);
    const float V = fmaxf(st[1] * (1.f / 576.f) - mean * mean, 0.f) + 1e-5f;
    const float rstd = 1.0f / sqrtf(V);
    const float C0 = -2.f * 0.7978845608028654f * 1.4426950408889634f * rstd, C1 = C0 * 0.044715f * rstd * rstd;
    char* adst = ws + OFF_ACT + ((size_t)tt * 288 + ft0) * 1024 + lane * 16;
    LAS const float* csw = csl + 4 * hh;
    const lptr ap = lds + lane * 16;
    f32x16 accP = {};
    for (int f = 0; f < 9; ++f) {
        if (f == 0) asm volatile("s_waitcnt vmcnt(44) lgkmcnt(0)\n\ts_barrier" ::: "memory");
        else asm volatile("s_waitcnt vmcnt(4) lgkmcnt(0)\n\ts_barrier" ::: "memory");
        if (f + 3 < 9) issue(f + 3);
        const lptr slot = ap + (f & 3) * K4_SLOT;
        if (wid >= 4 && f > 0) k4_epi(accP, csw + (f - 1) * 32, mean, C0, C1, V, adst + (size_t)(f - 1) * 1024);
        f32x16 acc = {};
#pragma unroll
        for (int k = 0; k < 36; ++k) acc = mfma32(lds128(slot + k * 1024), xf[k], acc);
        if (wid < 4) k4_epi(acc, csw + f * 32, mean, C0, C1, V, adst + (size_t)f * 1024);
        else accP = acc;
        SBAR();
    }
    if (wid >= 4) k4_epi(accP, csw + 8 * 32, mean, C0, C1, V, adst + (size_t)8 * 1024);
}

constexpr int K5_SLOT = 20 * 1024, K5_NS = 6;

__global__ __launch_bounds__(512, 2) void k5_down(char* __restrict__ ws) {
    __shared__ __attribute__((aligned(16))) char smem[K5_NS * K5_SLOT];
    const int tid = threadIdx.x, lane = tid & 63, wid = __builtin_amdgcn_readfirstlane(tid >> 6);
    const int bx = blockIdx.x;
    const int idx = (bx & 7) * 30 + (bx >> 3);
    const int sp = idx / 48, rem = idx % 48, m4 = rem / 3, dg = rem % 3;
    const int k0 = sp * 58, nst = sp < 4 ? 29 : 28;
    lptr lds = (lptr)smem;
    const unsigned ldsbase = (unsigned)(uintptr_t)lds;
    if (wid >= 4) {
        const int l = wid - 4;
        const char* src[5];
#pragma unroll
        for (int i = 0; i < 5; ++i) {
            const int p = 5 * l + i, kk = p / 10, q = p % 10;
            src[i] = q < 4 ? ws + OFF_ACT + ((size_t)(4 * m4 + q) * 288 + k0 + kk) * 1024
                           : ws + OFF_WDT + ((size_t)(6 * dg + q - 4) * 288 + k0 + kk) * 1024;
        }
        auto issue = [&](int s, int slot) {
#pragma unroll
            for (int i = 0; i < 5; ++i) dma16(lane * 16, src[i] + (size_t)s * 2048, ldsbase + slot * K5_SLOT + (5 * l + i) * 1024);
        };
        for (int s = 0; s < K5_NS - 1; ++s) issue(s, s);
        int slot = K5_NS - 1;
        for (int s = 0; s < nst; ++s) {
            const int issued = (s + K5_NS - 1 < nst ? s + K5_NS - 1 : nst);
            const int need = s + 2 < nst ? s + 2 : nst;
            const int fly = issued - need;
            if (fly >= 3) WAIT_VM(15); else if (fly == 2) WAIT_VM(10); else if (fly == 1) WAIT_VM(5); else WAIT_VM(0);
            __builtin_amdgcn_s_barrier();
            if (s + K5_NS - 1 < nst) { issue(s + K5_NS - 1, slot); }
            slot = slot == K5_NS - 1 ? 0 : slot + 1;
        }
        __builtin_amdgcn_s_barrier();
        return;
    }
    const int a = wid >> 1, b = wid & 1;
    const lptr bp = lds + (2 * a) * 1024 + lane * 16, apx = lds + (4 + 3 * b) * 1024 + lane * 16;
    f32x16 acc[2][3] = {};
    u32x4 B0[2], A0[3], B1[2], A1[3];
    auto rd = [&](u32x4 (&B)[2], u32x4 (&A)[3], int off) {
#pragma unroll
        for (int i = 0; i < 2; ++i) B[i] = lds128(bp + off + i * 1024);
#pragma unroll
        for (int i = 0; i < 3; ++i) A[i] = lds128(apx + off + i * 1024);
    };
    auto mm = [&](const u32x4 (&B)[2], const u32x4 (&A)[3]) {
#pragma unroll
        for (int i = 0; i < 2; ++i)
#pragma unroll
            for (int jx = 0; jx < 3; ++jx) acc[i][jx] = mfma32(A[jx], B[i], acc[i][jx]);
    };
    __builtin_amdgcn_s_barrier();
    asm volatile("" ::: "memory");
    rd(B0, A0, 0);
    int slot = 0;
    for (int s = 0; s < nst; ++s) {
        const int nslot = slot == K5_NS - 1 ? 0 : slot + 1;
        rd(B1, A1, slot * K5_SLOT + 10240);
        mm(B0, A0);
        if (s + 1 < nst) rd(B0, A0, nslot * K5_SLOT);
        mm(B1, A1);
        SBAR();
        asm volatile("s_barrier" ::: "memory");
        slot = nslot;
    }
    char* part = ws + OFF_PART + ((size_t)(sp * 48 + rem) * 4 + wid) * 24576 + lane * 16;
#pragma unroll
    for (int i = 0; i < 2; ++i)
#pragma unroll
        for (int jx = 0; jx < 3; ++jx)
#pragma unroll
            for (int q = 0; q < 4; ++q)
                *(f32x4*)(part + ((i * 3 + jx) * 4 + q) * 1024) = (f32x4){acc[i][jx][4 * q], acc[i][jx][4 * q + 1], acc[i][jx][4 * q + 2], acc[i][jx][4 * q + 3]};
}

__global__ __launch_bounds__(256) void k6_reduce(const char* __restrict__ ws, u16* __restrict__ out) {
    const int lane = threadIdx.x & 63, gw = blockIdx.x * 4 + (threadIdx.x >> 6);
    const int rem = gw / 24, u = gw % 24, w = u / 6, tl = u % 6, i = tl / 3, jx = tl % 3;
    const int m4 = rem / 3, dg = rem % 3, a = w >> 1, b = w & 1;
    const int tt = 4 * m4 + 2 * a + i, dT = 6 * dg + 3 * b + jx;
    const int hh = lane >> 5, l31 = lane & 31;
    float v[16];
#pragma unroll
    for (int r = 0; r < 16; ++r) v[r] = 0.f;
#pragma unroll
    for (int sp = 0; sp < 5; ++sp) {
        const char* part = ws + OFF_PART + ((size_t)(sp * 48 + rem) * 4 + w) * 24576 + tl * 4096 + lane * 16;
#pragma unroll
        for (int q = 0; q < 4; ++q) { const f32x4 p = *(const f32x4*)(part + q * 1024); v[4 * q] += p[0]; v[4 * q + 1] += p[1]; v[4 * q + 2] += p[2]; v[4 * q + 3] += p[3]; }
    }
    const char* ht = ws + OFF_HT + (size_t)(tt * 36 + 2 * dT) * 1024 + lane * 16;
    const u32x4 h0 = *(const u32x4*)ht, h1 = *(const u32x4*)(ht + 1024);
    u32x4 o0, o1;
#pragma unroll
    for (int q = 0; q < 4; ++q) {
        o0[q] = pk2(v[2 * q] + bflo(h0[q]), v[2 * q + 1] + bfhi(h0[q]));
        o1[q] = pk2(v[8 + 2 * q] + bflo(h1[q]), v[8 + 2 * q + 1] + bfhi(h1[q]));
    }
    u16* dst = out + (size_t)(32 * tt + l31) * D_ + 32 * dT + 8 * hh;
    *(u32x4*)dst = o0; *(u32x4*)(dst + 16) = o1;
}

extern "C" void kernel_launch(void* const* d_in, const int* in_sizes, int n_in, void* d_out, int out_size, void* d_ws, size_t ws_size,
                              hipStream_t stream) {
    const u16* x = (const u16*)d_in[0];
    const u16* wqkv = (const u16*)d_in[1];
    const u16* wo = (const u16*)d_in[2];
    const u16* wg = (const u16*)d_in[4];
    const u16* wu = (const u16*)d_in[5];
    const u16* wd = (const u16*)d_in[6];
    char* ws = (char*)d_ws;
    k1_qkv<<<256, 256, 0, stream>>>(x, wqkv, ws);
    k2_attn<<<256, 512, 0, stream>>>(wo, wg, wu, wd, ws);
    k3_wo<<<256, 320, 0, stream>>>(x, ws);
    k4_gateup<<<256, 512, 0, stream>>>(ws);
    k5_down<<<240, 512, 0, stream>>>(ws);
    k6_reduce<<<288, 256, 0, stream>>>(ws, (u16*)d_out);
}
```

```cpp
#include <hip/hip_runtime.h>
#include <stdint.h>
#include <type_traits>

typedef unsigned short u16;
typedef __attribute__((ext_vector_type(8))) __bf16 bf16x8;
typedef __attribute__((ext_vector_type(2))) __bf16 bf16x2;
typedef __attribute__((ext_vector_type(4))) short s16x4;
typedef __attribute__((ext_vector_type(16))) float f32x16;
typedef __attribute__((ext_vector_type(4))) float f32x4;
typedef __attribute__((ext_vector_type(2))) float f32x2;
typedef __attribute__((ext_vector_type(4))) unsigned u32x4;
typedef __attribute__((ext_vector_type(2))) unsigned u32x2;
#define LAS __attribute__((address_space(3)))
typedef LAS char* lptr;

constexpr int S_ = 2048, D_ = 576, F_ = 4608;
constexpr int QTILE = 1152, KVTILE = 2496;
constexpr size_t OFF_QIMG = 0;
constexpr size_t OFF_KV   = OFF_QIMG + (size_t)32 * 64 * QTILE;
constexpr size_t OFF_OT   = OFF_KV + (size_t)32 * 64 * KVTILE;
constexpr size_t OFF_HT   = OFF_OT + (size_t)64 * 36 * 1024;
constexpr size_t OFF_STAT = OFF_HT + (size_t)64 * 36 * 1024;
constexpr size_t OFF_WOT  = OFF_STAT + (size_t)4 * 2048 * 8;
constexpr size_t OFF_WGU  = OFF_WOT + (size_t)18 * 36 * 1024;
constexpr size_t OFF_CSP  = OFF_WGU + (size_t)288 * 36 * 1024;
constexpr size_t OFF_WDT  = OFF_CSP + (size_t)9 * 288 * 32 * 4;
constexpr size_t OFF_ACT  = OFF_WDT + (size_t)18 * 288 * 1024;
constexpr size_t OFF_PART = OFF_ACT + (size_t)64 * 288 * 1024;
constexpr size_t OFF_END  = OFF_PART + (size_t)240 * 98304;
constexpr size_t OFF_DBG  = (size_t)200 << 20;
#define TS(k, i)
#define TR(k, i)

__device__ __forceinline__ f32x16 mfma32(u32x4 a, u32x4 b, f32x16 c) {
    return __builtin_amdgcn_mfma_f32_32x32x16_bf16(__builtin_bit_cast(bf16x8, a), __builtin_bit_cast(bf16x8, b), c, 0, 0, 0);
}
__device__ __forceinline__ f32x16 mfma32k8(u32x2 a, u32x2 b, f32x16 c) {
    return __builtin_amdgcn_mfma_f32_32x32x8bf16_1k(__builtin_bit_cast(s16x4, a), __builtin_bit_cast(s16x4, b), c, 0, 0, 0);
}
__device__ __forceinline__ f32x4 mfma16(u32x4 a, u32x4 b, f32x4 c) {
    return __builtin_amdgcn_mfma_f32_16x16x32_bf16(__builtin_bit_cast(bf16x8, a), __builtin_bit_cast(bf16x8, b), c, 0, 0, 0);
}
__device__ __forceinline__ unsigned pk2(float lo, float hi) {
    f32x2 v = {lo, hi};
    return __builtin_bit_cast(unsigned, __builtin_convertvector(v, bf16x2));
}
__device__ __forceinline__ float bflo(unsigned u) { return __uint_as_float(u << 16); }
__device__ __forceinline__ float bfhi(unsigned u) { return __uint_as_float(u & 0xffff0000u); }
__device__ __forceinline__ void dma16(unsigned voff, const void* sbase, unsigned lds) {
    unsigned sv;
    asm volatile("s_mov_b32 %0, m0\n\ts_mov_b32 m0, %3\n\ts_nop 0\n\tglobal_load_lds_dwordx4 %1, %2\n\ts_mov_b32 m0, %0"
                 : "=&s"(sv) : "v"(voff), "s"(sbase), "s"(lds) : "memory");
}
#define WAIT_VM(n) asm volatile("s_waitcnt vmcnt(" #n ")" ::: "memory")
#define WAIT_VM_BAR(n) asm volatile("s_waitcnt vmcnt(" #n ") lgkmcnt(0)\n\ts_barrier" ::: "memory")
#define SBAR() __builtin_amdgcn_sched_barrier(0)
__device__ __forceinline__ u32x4 lds128(lptr p) { return *(LAS const u32x4*)p; }
__device__ __forceinline__ u32x2 lds64(lptr p) { return *(LAS const u32x2*)p; }
typedef short v4i16_t __attribute__((ext_vector_type(4)));
__device__ __forceinline__ u32x2 ldstr(lptr p) { return __builtin_bit_cast(u32x2, __builtin_amdgcn_ds_read_tr16_b64_v4i16((LAS v4i16_t*)p)); }
__device__ __forceinline__ float xhalf(float v) {
    return __shfl_xor(v, 32);
}
__device__ __forceinline__ int crow(int r, int hh) { return (r & 3) + 8 * (r >> 2) + 4 * hh; }

constexpr int K1_BUF = 32768, K1_NB = 4, K1_ST = K1_NB * K1_BUF;
constexpr float QSCALE = 0.23570226039551584f * 1.4426950408889634f;
#define PIN(x) asm volatile("" : "+v"(x))

struct K1Frag { u32x4 x0, x1, w0, w1; };
template <bool V0, bool V1, bool T0, bool T1>
__device__ __forceinline__ void k1_body(char* __restrict__ ws, lptr lds, int mt, int g, int wid, int lane) {
    const int a = wid >> 1, b = wid & 1;
    const int hh = lane >> 5, l31 = lane & 31;
    const int t0 = 2 * b + a, t1 = 2 * b + (1 - a);
    const int f0 = 2 * a + b, f1 = 2 * a + (1 - b);
    const int sw = (l31 >> 1) & 7;
    const int xc = (hh ^ sw) << 4;
    int xa0[4], xa1[4];
#pragma unroll
    for (int s = 0; s < 4; ++s) { xa0[s] = (32 * t0 + l31) * 128 + (xc ^ (s << 5)); xa1[s] = (32 * t1 + l31) * 128 + (xc ^ (s << 5)); }
    const int q4 = (lane >> 2) & 3, p4 = lane & 3, cg = (lane >> 4) & 1;
    auto waddr = [&](int f) {
        const int c = f < 3 ? 4 * f + 2 * cg + (p4 >> 1) : 12 + p4;
        const int sub = f < 3 ? 8 * (p4 & 1) : 0;
        return 16384 + (8 * hh + q4) * 256 + ((c ^ (q4 << 2)) << 4) + sub;
    };
    const int wa0 = waddr(f0), wa1 = waddr(f1);
    const u32x4 ones = {0x3F803F80u, 0x3F803F80u, 0x3F803F80u, 0x3F803F80u};
    const bool onesrow = l31 >= 12;

    f32x16 acc00 = {}, acc01 = {}, acc10 = {}, acc11 = {}, accG = {};
    f32x4 accC = {};
    const unsigned pick = ((lane & 15) == ((lane >> 4) & 1)) ? 0x3F803F80u : 0u;
    const u32x4 sumA = {pick, pick, pick, pick};
    auto step = [&](K1Frag& C, K1Frag& N, lptr nb, int nks) {
        u32x2 lo, hi;
        if (T0) { for (int i = 0; i < 4; ++i) C.w0[i] = onesrow ? ones[i] : C.w0[i]; }
        if (T1) { for (int i = 0; i < 4; ++i) C.w1[i] = onesrow ? ones[i] : C.w1[i]; }
        acc00 = V0 ? mfma32(C.x0, C.w0, acc00) : mfma32(C.w0, C.x0, acc00); PIN(acc00); SBAR();
        N.x0 = lds128(nb + xa0[nks]); SBAR();
        accG = mfma32(C.x0, C.x0, accG); PIN(accG); SBAR();
        lo = ldstr(nb + wa0 + nks * 4096); SBAR();
        acc01 = V0 ? mfma32(C.x1, C.w0, acc01) : mfma32(C.w0, C.x1, acc01); PIN(acc01); SBAR();
        hi = ldstr(nb + wa0 + nks * 4096 + 1024); SBAR();
        acc10 = V1 ? mfma32(C.x0, C.w1, acc10) : mfma32(C.w1, C.x0, acc10); PIN(acc10); SBAR();
        N.x1 = lds128(nb + xa1[nks]); SBAR();
        accC = mfma16(sumA, C.w0, accC); PIN(accC); SBAR();
        N.w0 = (u32x4){lo[0], lo[1], hi[0], hi[1]};
        lo = ldstr(nb + wa1 + nks * 4096); SBAR();
        acc11 = V1 ? mfma32(C.x1, C.w1, acc11) : mfma32(C.w1, C.x1, acc11); PIN(acc11); SBAR();
        hi = ldstr(nb + wa1 + nks * 4096 + 1024);
        N.w1 = (u32x4){lo[0], lo[1], hi[0], hi[1]};
        SBAR();
    };
    K1Frag F0, F1, F2, F3;
    auto rdall = [&](K1Frag& N, int ks) {
        N.x0 = lds128(lds + xa0[ks]); N.x1 = lds128(lds + xa1[ks]);
        const u32x2 a0 = ldstr(lds + wa0 + ks * 4096), a1 = ldstr(lds + wa0 + ks * 4096 + 1024), b0 = ldstr(lds + wa1 + ks * 4096), b1 = ldstr(lds + wa1 + ks * 4096 + 1024);
        N.w0 = (u32x4){a0[0], a0[1], a1[0], a1[1]}; N.w1 = (u32x4){b0[0], b0[1], b1[0], b1[1]};
    };
    asm volatile("s_barrier" ::: "memory");
    rdall(F0, 0); rdall(F1, 1);
    for (int s = 0; s < 9; ++s) {
        lptr buf = lds + (s & 3) * K1_BUF, nbuf = lds + ((s + 1) & 3) * K1_BUF;
        step(F0, F2, buf, 2);
        step(F1, F3, buf, 3);
        asm volatile("s_barrier" ::: "memory");
        step(F2, F0, nbuf, 0);
        step(F3, F1, nbuf, 1);
    }
    TS(1, 3);
    LAS float* st = (LAS float*)(lds + K1_ST);
    LAS float* cs = (LAS float*)(lds + K1_ST + 1024);
    {
        float dg = accG[0];
        const int rsel = (l31 & 3) + 4 * (l31 >> 3);
#pragma unroll
        for (int r = 1; r < 16; ++r) dg = rsel == r ? accG[r] : dg;
        if (hh == ((l31 >> 2) & 1)) st[(32 * t0 + l31) * 2 + 1] = dg;
        if (lane < 16) { cs[32 * f0 + lane] = accC[0]; cs[32 * f0 + 16 + lane] = accC[1]; }
        if (T0 && hh == 1) { st[(32 * t0 + l31) * 2] = acc00[4]; st[(32 * t1 + l31) * 2] = acc01[4]; }
        if (T1 && hh == 1) { st[(32 * t0 + l31) * 2] = acc10[4]; st[(32 * t1 + l31) * 2] = acc11[4]; }
    }
    asm volatile("s_waitcnt lgkmcnt(0)\n\ts_barrier" ::: "memory");
    {
        const f32x2 sv = *(LAS const f32x2*)(st + (32 * wid + l31) * 2);
        const float mean = sv[0] * (1.f / 576.f);
        const float var = fmaxf(sv[1] * (1.f / 576.f) - mean * mean, 0.f);
        const float rstd = 1.0f / sqrtf(var + 1e-5f);
        if (hh == 0) *(LAS f32x2*)(st + 512 + (32 * wid + l31) * 2) = (f32x2){rstd, mean * rstd};
    }
    asm volatile("s_waitcnt lgkmcnt(0)\n\ts_barrier" ::: "memory");
    st += 512;
    TS(1, 4);
    char* qimg = ws + OFF_QIMG; char* kv = ws + OFF_KV;
    auto finish = [&](const f32x16& acc, int f, int t, bool isV) {
        const int tt = 4 * mt + t;
        float v[16];
        if (f < 3 && isV) {
            const int P = 3 * g + f, pp = P & 15;
            const float c = cs[32 * f + l31];
#pragma unroll
            for (int r = 0; r < 16; ++r) {
                const f32x2 rm = *(LAS const f32x2*)(st + (32 * t + crow(r, hh)) * 2);
                v[r] = acc[r] * rm[0] - rm[1] * c;
            }
            const int h = 2 * pp + (l31 >> 4), d = l31 & 15;
            char* dst = kv + (size_t)(h * 64 + tt) * KVTILE + 1280 + hh * 304 + d * 16;
            *(u32x4*)dst = (u32x4){pk2(v[0], v[1]), pk2(v[2], v[3]), pk2(v[4], v[5]), pk2(v[6], v[7])};
            *(u32x4*)(dst + 608) = (u32x4){pk2(v[8], v[9]), pk2(v[10], v[11]), pk2(v[12], v[13]), pk2(v[14], v[15])};
            return;
        }
        const f32x2 rm = *(LAS const f32x2*)(st + (32 * t + l31) * 2);
#pragma unroll
        for (int r = 0; r < 16; ++r) v[r] = acc[r] * rm[0] - rm[1] * cs[32 * f + crow(r, hh)];
        if (f < 3) {
            const int P = 3 * g + f, sec = P >> 4, pp = P & 15;
            const float sc = sec == 0 ? QSCALE : 1.f;
            char* base = sec == 0 ? qimg + (size_t)(2 * pp * 64 + tt) * QTILE : kv + (size_t)(2 * pp * 64 + tt) * KVTILE;
            const size_t hs = sec == 0 ? (size_t)64 * QTILE : (size_t)64 * KVTILE;
            *(u32x4*)(base + lane * 16) = (u32x4){pk2(v[0] * sc, v[1] * sc), pk2(v[2] * sc, v[3] * sc), pk2(v[4] * sc, v[5] * sc), pk2(v[6] * sc, v[7] * sc)};
            *(u32x4*)(base + hs + lane * 16) = (u32x4){pk2(v[8] * sc, v[9] * sc), pk2(v[10] * sc, v[11] * sc), pk2(v[12] * sc, v[13] * sc), pk2(v[14] * sc, v[15] * sc)};
        } else {
#pragma unroll
            for (int u = 0; u < 2; ++u) {
                const int p = 2 * u + hh;
                if (p < 3) {
                    const int P = 3 * g + p, sec = P >> 4, pp = P & 15;
#pragma unroll
                    for (int o = 0; o < 2; ++o) {
                        const int h = 2 * pp + o;
                        const float e0 = v[4 * u + 2 * o], e1 = v[4 * u + 2 * o + 1];
                        if (sec == 0) {
                            *(unsigned*)(qimg + (size_t)(h * 64 + tt) * QTILE + 1024 + l31 * 4) = pk2(e0 * QSCALE, e1 * QSCALE);
                        } else if (sec == 1) {
                            const unsigned pk = pk2(e0, e1);
                            *(u32x2*)(kv + (size_t)(h * 64 + tt) * KVTILE + 1024 + l31 * 8) = (u32x2){pk, pk};
                        } else {
                            const unsigned pk = pk2(e0, e1);
                            const int off = (2 * (l31 >> 4) + ((l31 >> 2) & 1)) * 304 + (((l31 >> 3) & 1) * 4 + (l31 & 3)) * 2;
                            char* dst = kv + (size_t)(h * 64 + tt) * KVTILE + 1280 + off;
                            *(u16*)(dst + 16 * 16) = (u16)(pk & 0xffff);
                            *(u16*)(dst + 17 * 16) = (u16)(pk >> 16);
                            *(u16*)(dst + 18 * 16) = (u16)0x3F80;
                        }
                    }
                }
            }
        }
    };
    finish(acc00, f0, t0, V0); finish(acc01, f0, t1, V0); finish(acc10, f1, t0, V1); finish(acc11, f1, t1, V1);
    TS(1, 5); TR(1, 15);
}

__global__ __launch_bounds__(512, 2) void k1_qkv(const u16* __restrict__ x, const u16* __restrict__ w, char* __restrict__ ws) {
    __shared__ __attribute__((aligned(16))) char smem[K1_ST + 4096];
    const int lane = threadIdx.x & 63, wid = __builtin_amdgcn_readfirstlane(threadIdx.x >> 6);
    const int bx = blockIdx.x, xcd = bx & 7, j = bx >> 3;
    const int mt = (xcd >> 1) * 4 + (j & 3), g = (xcd & 1) * 8 + (j >> 2);
    lptr lds = (lptr)smem;
    TS(1, 0); TR(1, 14);
    if (wid >= 4) {
        const int l = wid - 4;
        const unsigned ldsbase = (unsigned)(uintptr_t)lds;
        unsigned xoff[4], woff[4];
#pragma unroll
        for (int i = 0; i < 4; ++i) {
            const int p = 4 * l + i;
            const int row = 8 * p + (lane >> 3), ch = (lane & 7) ^ ((row >> 1) & 7);
            xoff[i] = row * 1152 + ch * 16;
            const int kr = 4 * p + (lane >> 4), c = (lane & 15) ^ ((kr & 3) << 2);
            const int cs = c & 3;
            const int byte = c < 12 ? 72 * (c >> 2) + (cs < 2 ? 16 * cs : 40 + 16 * (cs - 2)) : 72 * ((c < 14 ? c : 14) - 12) + 32;
            woff[i] = kr * 3456 + byte;
        }
        const char* xb = (const char*)x + (size_t)mt * 128 * 1152;
        const char* wb = (const char*)w + g * 216;
#define K1_ROT 0
        const int rot = K1_ROT ? ((j >> 2) + 2 * (j & 3)) % 9 : 0;
        auto issue = [&](int s) {
            const unsigned dst = ldsbase + (s & 3) * K1_BUF + l * 4096;
            const int ks = s + rot < 9 ? s + rot : s + rot - 9;
#pragma unroll
            for (int i = 0; i < 4; ++i) {
                dma16(xoff[i], xb + ks * 128, dst + i * 1024);
                dma16(woff[i], wb + (size_t)ks * 64 * 3456, dst + 16384 + i * 1024);
            }
        };
        issue(0); issue(1); issue(2);
        TS(1, 1);
        for (int s = 0; s < 9; ++s) {
            if (s == 0) WAIT_VM(16); else if (s < 8) WAIT_VM(8); else WAIT_VM(0);
            __builtin_amdgcn_s_barrier();
            if (s == 0) TS(1, 2);
            if (s >= 1 && s < 7) issue(s + 2);
        }
        __builtin_amdgcn_s_barrier();
        __builtin_amdgcn_s_barrier(); __builtin_amdgcn_s_barrier();
        return;
    }
    const int a = wid >> 1, b = wid & 1;
    const int f0 = 2 * a + b;
    const bool vA = 3 * g >= 32, vB = 3 * g + 2 >= 32;
    if (a == 0) { if (vA) k1_body<true, true, false, false>(ws, lds, mt, g, wid, lane); else k1_body<false, false, false, false>(ws, lds, mt, g, wid, lane); }
    else if (f0 == 2) { if (vB) k1_body<true, false, false, true>(ws, lds, mt, g, wid, lane); else k1_body<false, false, false, true>(ws, lds, mt, g, wid, lane); }
    else { if (vB) k1_body<false, true, true, false>(ws, lds, mt, g, wid, lane); else k1_body<false, false, true, false>(ws, lds, mt, g, wid, lane); }
}

__device__ __forceinline__ int phi32(int rho) { const int r = (rho & 3) + 4 * (rho >> 3), hc = (rho >> 2) & 1; return 16 * (r >> 3) + 8 * hc + (r & 7); }
struct SideW { const u16 *wo, *wg, *wu, *wd; char* ws; };
struct SideJob {
    int type;
    const char* src[2];
    int pstep[4];
    unsigned voff;
    char* dst[2]; int dstep[4];
    int raddr[4];
    float* cs;
};
__device__ __forceinline__ SideJob side_job(const SideW& w, int item, int lane) {
    SideJob g;
    const int q4 = (lane >> 2) & 3, p4 = lane & 3, grp = lane >> 4;
    g.cs = nullptr; g.type = 3; g.voff = 0;
    int rs = D_ * 2;
    if (item < 1296) {
        const int U = item / 18, mat = (item % 18) / 9, sp = item % 9;
        g.type = 0; rs = F_ * 2;
        g.src[0] = (const char*)(mat ? w.wu : w.wg) + 128 * U + (size_t)(64 * sp) * rs; g.src[1] = g.src[0] + 32 * rs;
        g.dst[0] = w.ws + OFF_WGU + (size_t)((4 * U * 18 + 2 * sp) * 2 + mat) * 1024; g.dst[1] = g.dst[0] + 2048;
        g.cs = (float*)(w.ws + OFF_CSP) + (size_t)(sp * 288 + 4 * U) * 32 + mat * 16;
#pragma unroll
        for (int t = 0; t < 4; ++t) { g.pstep[t] = 8 * t * rs; g.dstep[t] = t * 36 * 1024; g.raddr[t] = (8 * grp + q4) * 128 + 32 * t + 8 * p4; }
        g.voff = (lane >> 3) * rs + (lane & 7) * 16;
    } else if (item < 1944) {
        const int it = item - 1296, V = it / 72, sp = it % 72;
        g.type = 1;
        g.src[0] = (const char*)w.wd + 128 * V + (size_t)(64 * sp) * rs; g.src[1] = g.src[0] + 32 * rs;
        g.dst[0] = w.ws + OFF_WDT + (size_t)(((V / 3) * 144 + 2 * sp) * 12 + (V % 3) * 4) * 1024; g.dst[1] = g.dst[0] + 12 * 1024;
#pragma unroll
        for (int t = 0; t < 4; ++t) { g.pstep[t] = 8 * t * rs; g.dstep[t] = t * 1024; g.raddr[t] = (8 * grp + q4) * 128 + 64 * (t >> 1) + 16 * p4 + 8 * (t & 1); }
        g.voff = (lane >> 3) * rs + (lane & 7) * 16;
    } else {
        const int it = item < 2025 ? item - 1944 : 0, V = it / 9, kq = it % 9;
        const bool mainp = kq < 8;
        const int jj = lane >> 3;
        if (item < 2025) g.type = 2;
#pragma unroll
        for (int b = 0; b < 2; ++b) {
            const int ks = 4 * kq + 2 * b;
            g.src[b] = (const char*)w.wo + 128 * V + (size_t)(mainp ? 18 * ks : 144 * (ks - 32)) * rs;
            g.dst[b] = w.ws + OFF_WOT + (size_t)(2 * V * 36 + ks) * 1024;
        }
#pragma unroll
        for (int t = 0; t < 4; ++t) {
            g.pstep[t] = (mainp ? 20 * (t >> 1) + 4 * (t & 1) : 144 * (t >> 1) + 72 * (t & 1)) * rs;
            g.dstep[t] = (t >> 1) * 1024 + (t & 1) * 36 * 1024;
            g.raddr[t] = (16 * (t >> 1) + 8 * (grp >> 1) + q4) * 128 + 64 * (t & 1) + 2 * phi32(16 * (grp & 1) + 4 * p4);
        }
        g.voff = (mainp ? 8 * (jj >> 2) + (jj & 3) : 18 * (jj >> 1) + (((jj >> 1) & 1) ? (jj & 1) : 16 + (jj & 1))) * rs + (lane & 7) * 16;
    }
    return g;
}
__device__ __forceinline__ void side_fetch(const SideJob& g, int b, unsigned ldsbuf) {
    if (g.type != 3) {
#pragma unroll
        for (int p = 0; p < 4; ++p) dma16(g.voff, g.src[b] + g.pstep[p], ldsbuf + p * 1024);
    }
}
__device__ __forceinline__ void side_finish(const SideJob& g, int b, lptr buf, f32x4 (&cs)[4], int lane) {
    if (g.type == 3) return;
    u32x4 v[4];
#pragma unroll
    for (int t = 0; t < 4; ++t) {
        const u32x2 lo = ldstr(buf + g.raddr[t]), hi = ldstr(buf + g.raddr[t] + 512);
        v[t] = (u32x4){lo[0], lo[1], hi[0], hi[1]};
    }
#pragma unroll
    for (int t = 0; t < 4; ++t) *(u32x4*)(g.dst[b] + g.dstep[t] + lane * 16) = v[t];
    if (g.type == 0) {
        const u32x4 ones = {0x3F803F80u, 0x3F803F80u, 0x3F803F80u, 0x3F803F80u};
#pragma unroll
        for (int t = 0; t < 4; ++t) cs[t] = mfma16(v[t], ones, cs[t]);
        if (b == 1 && (lane & 15) == 0) {
#pragma unroll
            for (int t = 0; t < 4; ++t) *(f32x4*)(g.cs + t * 32 + (lane >> 4) * 4) = cs[t];
        }
    }
}

constexpr int K2_LDS = 64 * KVTILE;

struct K2Regs { u32x4 km, v0, v1; u32x2 kt; float e0, e1; };
struct NoHook { __device__ __forceinline__ void operator()(int) const {} };
template <bool QKN, bool LDKN, bool LDVN, class H>
__device__ __forceinline__ void k2_step(f32x16& SC, f32x16& SN, f32x16& o, K2Regs& R, const u32x4& qm, const u32x2& qtf,
                                        lptr kp, lptr ktp, lptr vp, H&& hook) {
    float e[16]; unsigned pw[8];
#define EX(i) do { e[i] = __builtin_amdgcn_exp2f(SC[i]); PIN(e[i]); } while (0)
#define CV(i) do { pw[i] = pk2(e[2 * (i)], e[2 * (i) + 1]); PIN(pw[i]); } while (0)
    e[0] = R.e0; e[1] = R.e1;
    EX(2); EX(3); EX(4); EX(5);
    SBAR();
    if constexpr (QKN) { SN = mfma32k8(R.kt, qtf, (f32x16){}); PIN(SN); SBAR(); }
    hook(0);
    EX(6); EX(7); CV(0); CV(1); CV(2); CV(3);
    SBAR();
    if constexpr (QKN) { SN = mfma32(R.km, qm, SN); PIN(SN); SBAR(); }
    if constexpr (LDKN) { R.km = lds128(kp + 2 * KVTILE); R.kt = lds64(ktp + 2 * KVTILE); }
    EX(8); EX(9); EX(10); EX(11);
    SBAR();
    o = mfma32(R.v0, (u32x4){pw[0], pw[1], pw[2], pw[3]}, o); PIN(o); SBAR();
    if constexpr (LDVN) R.v0 = lds128(vp + KVTILE);
    hook(1);
    EX(12); EX(13); EX(14); EX(15); CV(4); CV(5); CV(6); CV(7);
    SBAR();
    if constexpr (QKN) { R.e0 = __builtin_amdgcn_exp2f(SN[0]); PIN(R.e0); R.e1 = __builtin_amdgcn_exp2f(SN[1]); PIN(R.e1); }
    else { asm volatile("s_nop 1"); }
    SBAR();
    o = mfma32(R.v1, (u32x4){pw[4], pw[5], pw[6], pw[7]}, o); PIN(o); SBAR();
    if constexpr (LDVN) R.v1 = lds128(vp + KVTILE + 608);
    SBAR();
#undef EX
#undef CV
}

__global__ __launch_bounds__(512, 2) void k2_attn(const u16* __restrict__ wo, const u16* __restrict__ wg, const u16* __restrict__ wu,
                                                  const u16* __restrict__ wd, char* __restrict__ ws) {
    __shared__ __attribute__((aligned(16))) char smem[K2_LDS + 256];
    const int lane = threadIdx.x & 63, wid = __builtin_amdgcn_readfirstlane(threadIdx.x >> 6);
    const int bx = blockIdx.x, xcd = bx & 7, j = bx >> 3;
    const int h = 4 * xcd + (j >> 3), qb = j & 7, tt = 8 * qb + wid;
    const int hh = lane >> 5, l31 = lane & 31;
    lptr lds = (lptr)smem;
    const unsigned ldsbase = (unsigned)(uintptr_t)lds;
    TS(2, 0); TR(2, 14);
    const unsigned voff = lane * 16;
    const char* kvsrc = ws + OFF_KV + (size_t)h * K2_LDS;
    {
        const char* s0 = kvsrc + wid * 5120; const unsigned d0 = ldsbase + wid * 5120;
        asm volatile("s_mov_b32 m0, %2\n\ts_nop 0\n\tglobal_load_lds_dwordx4 %0, %1" :: "v"(voff), "s"(s0), "s"(d0) : "memory");
    }
    u32x4 qm; unsigned qt;
    {
        const char* qsrc = ws + OFF_QIMG + (size_t)(h * 64 + tt) * QTILE;
        const unsigned o2 = 1024 + l31 * 4;
        asm volatile("global_load_dwordx4 %0, %2, %4\n\tglobal_load_dword %1, %3, %4" : "=&v"(qm), "=&v"(qt) : "v"(voff), "v"(o2), "s"(qsrc) : "memory");
    }
    {
        const char* s0 = kvsrc + wid * 5120; const unsigned d0 = ldsbase + wid * 5120;
        asm volatile("global_load_lds_dwordx4 %0, %1 offset:1024\n\tglobal_load_lds_dwordx4 %0, %1 offset:2048\n\tglobal_load_lds_dwordx4 %0, %1 offset:3072"
                     :: "v"(voff), "s"(s0) : "memory");
        dma16(voff, s0 + 4096, d0 + 4096);
    }
    auto piece = [&](int c, int i) {
        int off = 40960 + 32768 * c + 4096 * wid;
        off = off + 4096 <= K2_LDS ? off : K2_LDS - 4096;
        if (i == 0) asm volatile("s_mov_b32 m0, %2\n\ts_nop 0\n\tglobal_load_lds_dwordx4 %0, %1" :: "v"(voff), "s"(kvsrc + off), "s"(ldsbase + off) : "memory");
        else if (i == 1) asm volatile("global_load_lds_dwordx4 %0, %1 offset:1024" :: "v"(voff), "s"(kvsrc + off) : "memory");
        else if (i == 2) asm volatile("global_load_lds_dwordx4 %0, %1 offset:2048" :: "v"(voff), "s"(kvsrc + off) : "memory");
        else asm volatile("global_load_lds_dwordx4 %0, %1 offset:3072" :: "v"(voff), "s"(kvsrc + off) : "memory");
    };
    lptr kp = lds + lane * 16, ktp = lds + 1024 + l31 * 8, vp = lds + 1280 + hh * 304 + l31 * 16;
    const bool young = wid >= 4;
#define PRIO(p) do { if (young) __builtin_amdgcn_s_setprio(p); } while (0)
    const SideW sw{wo, wg, wu, wd, ws};
    const SideJob job = side_job(sw, bx * 8 + wid, lane);
    asm volatile("s_waitcnt vmcnt(0)\n\ts_barrier" : "+v"(qm), "+v"(qt) :: "memory");
    TS(2, 1);
    PRIO(1);
    const u32x2 qtf = {hh == 0 ? qt : 0u, 0u};
    f32x16 o = {}, sA, sB;
    K2Regs R;
    R.km = lds128(kp); R.kt = lds64(ktp);
    sA = mfma32(R.km, qm, mfma32k8(R.kt, qtf, (f32x16){}));
    R.km = lds128(kp + KVTILE); R.kt = lds64(ktp + KVTILE);
    R.v0 = lds128(vp); R.v1 = lds128(vp + 608);
    R.e0 = __builtin_amdgcn_exp2f(sA[0]); R.e1 = __builtin_amdgcn_exp2f(sA[1]);
#define TWO(HA, HB) do { \
        k2_step<true, true, true>(sA, sB, o, R, qm, qtf, kp, ktp, vp, HA); \
        k2_step<true, true, true>(sB, sA, o, R, qm, qtf, kp + KVTILE, ktp + KVTILE, vp + KVTILE, HB); \
        kp += 2 * KVTILE; ktp += 2 * KVTILE; vp += 2 * KVTILE; } while (0)
    const NoHook nh;
#define TWO_DMA(c) TWO([&](int p) { piece(c, p); }, [&](int p) { piece(c, 2 + p); })
    TWO_DMA(0); TWO_DMA(1); TWO_DMA(2); TWO_DMA(3);
    TWO(nh, nh);
#define K2_SW0 10
    if (K2_SW0 == 10) PRIO(0);
    TWO(nh, nh);
    if (K2_SW0 == 12) PRIO(0);
    TWO(nh, nh);
    asm volatile("s_waitcnt vmcnt(0)\n\ts_barrier" ::: "memory");
    PRIO(1);
    TS(2, 2);
    const lptr sbuf = lds + wid * 4096;
    f32x4 cs[4] = {};
    side_fetch(job, 0, ldsbase + wid * 4096);
    int t = 14;
    for (; t < 20; t += 2) TWO(nh, nh);
    WAIT_VM(0);
    side_finish(job, 0, sbuf, cs, lane);
    asm volatile("s_waitcnt lgkmcnt(0)" ::: "memory");
    side_fetch(job, 1, ldsbase + wid * 4096);
    for (; t < 26; t += 2) TWO(nh, nh);
    asm volatile("s_waitcnt vmcnt(0)" ::: "memory");
    side_finish(job, 1, sbuf, cs, lane);
    TS(2, 3);
#define K2_SW 52
    for (; t < K2_SW; t += 2) TWO(nh, nh);
    PRIO(0);
    for (; t < 60; t += 2) TWO(nh, nh);
    k2_step<true, true, true>(sA, sB, o, R, qm, qtf, kp, ktp, vp, nh);
    k2_step<true, true, true>(sB, sA, o, R, qm, qtf, kp + KVTILE, ktp + KVTILE, vp + KVTILE, nh);
    k2_step<true, false, true>(sA, sB, o, R, qm, qtf, kp + 2 * KVTILE, ktp + 2 * KVTILE, vp + 2 * KVTILE, nh);
    k2_step<false, false, false>(sB, sA, o, R, qm, qtf, kp + 3 * KVTILE, ktp + 3 * KVTILE, vp + 3 * KVTILE, nh);
#undef TWO
#undef TWO_DMA
#undef PRIO
    TS(2, 4);
    float l = __shfl(o[10], l31);
    const bool bad = !(l > 8.673617379884035e-19f && l < 1.152921504606847e18f);
    if (__builtin_expect(__any(bad), 0)) {
        o = (f32x16){}; float m = -3.0e38f;
        const char* kq = kvsrc + lane * 16; const char* ktq = kvsrc + 1024 + l31 * 8; const char* vq = kvsrc + 1280 + hh * 304 + (l31 < 18 ? l31 : 18) * 16;
        for (int tb = 0; tb < 64; ++tb) {
            const u32x4 km = *(const u32x4*)(kq + tb * KVTILE), v0 = *(const u32x4*)(vq + tb * KVTILE), v1 = *(const u32x4*)(vq + tb * KVTILE + 608);
            const u32x2 kt = *(const u32x2*)(ktq + tb * KVTILE);
            const f32x16 s = mfma32(km, qm, mfma32k8(kt, qtf, (f32x16){}));
            float mx = s[0];
#pragma unroll
            for (int r = 1; r < 16; ++r) mx = fmaxf(mx, s[r]);
            mx = fmaxf(mx, xhalf(mx));
            const float mn = fmaxf(m, mx), al = __builtin_amdgcn_exp2f(m - mn);
            m = mn;
            unsigned pw[8];
#pragma unroll
            for (int i = 0; i < 8; ++i) pw[i] = pk2(__builtin_amdgcn_exp2f(s[2 * i] - mn), __builtin_amdgcn_exp2f(s[2 * i + 1] - mn));
#pragma unroll
            for (int r = 0; r < 16; ++r) o[r] *= al;
            o = mfma32(v0, (u32x4){pw[0], pw[1], pw[2], pw[3]}, o);
            o = mfma32(v1, (u32x4){pw[4], pw[5], pw[6], pw[7]}, o);
        }
        l = __shfl(o[10], l31);
    }
    const float inv = 1.0f / l;
    char* ot = ws + OFF_OT + (size_t)tt * 36 * 1024;
    *(u32x4*)(ot + h * 1024 + lane * 16) = (u32x4){pk2(o[0] * inv, o[1] * inv), pk2(o[2] * inv, o[3] * inv), pk2(o[4] * inv, o[5] * inv), pk2(o[6] * inv, o[7] * inv)};
    if (hh == 0) *(unsigned*)(ot + (32 + (h >> 3)) * 1024 + (((h & 7) >> 2) * 32 + l31) * 16 + (h & 3) * 4) = pk2(o[8] * inv, o[9] * inv);
    TS(2, 5); TR(2, 15);
}

__global__ __launch_bounds__(320, 1) void k3_wo(const u16* __restrict__ x, char* __restrict__ ws) {
    __shared__ __attribute__((aligned(16))) char smem[36 * 1024 + 5 * 32 * 8];
    const int lane = threadIdx.x & 63, wid = __builtin_amdgcn_readfirstlane(threadIdx.x >> 6);
    const int bx = blockIdx.x, tt = (bx & 7) * 8 + ((bx >> 3) & 7), cg = bx >> 6;
    const int hh = lane >> 5, l31 = lane & 31;
    const int dT0 = cg < 2 ? 5 * cg : 10 + 4 * (cg - 2), nT = cg < 2 ? 5 : 4;
    lptr lds = (lptr)smem;
    const unsigned ldsbase = (unsigned)(uintptr_t)lds;
    TS(3, 0); TR(3, 14);
    const char* osrc = ws + OFF_OT + (size_t)tt * 36 * 1024;
#pragma unroll
    for (int i = 0; i < 8; ++i) { int p = wid + 5 * i; p = p < 36 ? p : 35; dma16(lane * 16, osrc + p * 1024, ldsbase + p * 1024); }
    const bool act = wid < nT;
    const int dT = dT0 + (act ? wid : 0);
    const u32x4* wsrc = (const u32x4*)(ws + OFF_WOT + (size_t)dT * 36 * 1024) + lane;
    u32x4 af[36];
#pragma unroll
    for (int k = 0; k < 36; ++k) af[k] = wsrc[k * 64];
    const u16* xr = x + (size_t)(32 * tt + l31) * D_ + 32 * dT + 8 * hh;
    const u32x4 xr0 = *(const u32x4*)xr, xr1 = *(const u32x4*)(xr + 16);
    TS(3, 1);
    asm volatile("s_waitcnt vmcnt(38)\n\ts_barrier" ::: "memory");
    TS(3, 2);
    f32x16 acc = {};
#pragma unroll
    for (int k = 0; k < 36; ++k) acc = mfma32(af[k], lds128(lds + k * 1024 + lane * 16), acc);
    TS(3, 3);
    float s1 = 0.f, s2 = 0.f;
    unsigned hp[8];
#pragma unroll
    for (int i = 0; i < 8; ++i) {
        const unsigned xv = i < 4 ? xr0[i] : xr1[i - 4];
        hp[i] = pk2(acc[2 * i] + bflo(xv), acc[2 * i + 1] + bfhi(xv));
        const float a = bflo(hp[i]), b = bfhi(hp[i]);
        s1 += a + b; s2 += a * a + b * b;
    }
    s1 += xhalf(s1); s2 += xhalf(s2);
    LAS f32x2* red = (LAS f32x2*)(lds + 36 * 1024);
    if (act) {
        char* ht = ws + OFF_HT + (size_t)(tt * 36 + 2 * dT) * 1024 + lane * 16;
        *(u32x4*)ht = (u32x4){hp[0], hp[1], hp[2], hp[3]};
        *(u32x4*)(ht + 1024) = (u32x4){hp[4], hp[5], hp[6], hp[7]};
    }
    if (hh == 0) red[wid * 32 + l31] = act ? (f32x2){s1, s2} : (f32x2){0.f, 0.f};
    __syncthreads();
    if (threadIdx.x < 32) {
        f32x2 r = red[l31];
#pragma unroll
        for (int i = 1; i < 5; ++i) r += red[i * 32 + l31];
        *(f32x2*)(ws + OFF_STAT + (size_t)(cg * 2048 + 32 * tt + l31) * 8) = r;
    }
    TS(3, 4); TR(3, 15);
}

constexpr int K4_SLOT = 36 * 1024, K4_CS = 4 * K4_SLOT, K4_NB = 4, K4_XD = 8;

struct K4Acc { f32x4 g[2], u[2]; };
struct K4Epi { float g[8], u[8], e[8]; unsigned pk[4]; f32x4 cg, cu; };
struct K4Ctx {
    lptr slot[4]; LAS const float* csw; float m[2], C0[2], C1[2], V[2];
    const char* wsrc; unsigned ldsbase; int wid; unsigned voff, voffw, voffx, voffa; const char* hsrc; char* adst;
};
template <int R, int OP>
__device__ __forceinline__ void k4_epi_op(K4Epi& E, const K4Acc& acc, const K4Ctx& c) {
    constexpr int U = R >> 2, Q = R & 3;
    if constexpr (OP == 0) { E.g[R] = __builtin_fmaf(-c.m[U], E.cg[Q], acc.g[U][Q]); PIN(E.g[R]); }
    else if constexpr (OP == 1) { E.u[R] = __builtin_fmaf(-c.m[U], E.cu[Q], acc.u[U][Q]); PIN(E.u[R]); }
    else if constexpr (OP == 2) { E.e[R] = E.g[R] * E.g[R]; PIN(E.e[R]); }
    else if constexpr (OP == 3) { E.e[R] = __builtin_fmaf(E.e[R], c.C1[U], c.C0[U]); PIN(E.e[R]); }
    else if constexpr (OP == 4) { E.e[R] = E.g[R] * E.e[R]; PIN(E.e[R]); }
    else if constexpr (OP == 5) { E.e[R] = __builtin_amdgcn_exp2f(E.e[R]); PIN(E.e[R]); }
    else if constexpr (OP == 6) { E.e[R] = __builtin_fmaf(E.e[R], c.V[U], c.V[U]); PIN(E.e[R]); }
    else if constexpr (OP == 7) { E.e[R] = __builtin_amdgcn_rcpf(E.e[R]); PIN(E.e[R]); }
    else if constexpr (OP == 8) { E.u[R] = E.g[R] * E.u[R]; PIN(E.u[R]); }
    else if constexpr (OP == 9) { E.u[R] = E.u[R] * E.e[R]; PIN(E.u[R]); }
    else if constexpr (R & 1) { E.pk[R >> 1] = pk2(E.u[R - 1], E.u[R]); PIN(E.pk[R >> 1]); }
}
template <int M, int R>
__device__ __forceinline__ void k4_epi_slot(K4Epi& E, const K4Acc& acc, const K4Ctx& c) {
    if constexpr (R < 8) {
        constexpr int d = M - (6 + 6 * R);
        if constexpr (d >= 0 && d <= 20 && (d & 1) == 0) k4_epi_op<R, d / 2>(E, acc, c);
        k4_epi_slot<M, R + 1>(E, acc, c);
    }
}
__device__ __forceinline__ void k4_epi_begin(K4Epi& E, const K4Ctx& c, int f) {
    LAS const float* cs = c.csw + f * 32;
    E.cg = *(LAS const f32x4*)(cs); E.cu = *(LAS const f32x4*)(cs + 16);
}
template <int U>
__device__ __forceinline__ void k4_store(const K4Epi& E, const K4Ctx& c, int f) {
    *(u32x2*)(c.adst + (size_t)f * 1024 + U * 256 + c.voffa) = (u32x2){E.pk[2 * U], E.pk[2 * U + 1]};
}
template <int I>
__device__ __forceinline__ void k4_dma(const K4Ctx& c, int f) {
    const char* src = c.wsrc + (size_t)f * K4_SLOT;
    if constexpr (I == 0)
        asm volatile("s_mov_b32 m0, %2\n\ts_nop 0\n\tglobal_load_lds_dwordx4 %0, %1" :: "v"(c.voffw), "s"(src), "s"(c.ldsbase + (f & 3) * K4_SLOT + c.wid * 4096) : "memory");
    else if constexpr (I < 4)
        asm volatile("global_load_lds_dwordx4 %0, %1 offset:%2" :: "v"(c.voffw), "s"(src), "i"(I * 1024) : "memory");
    else if (c.wid < 4)
        asm volatile("s_mov_b32 m0, %2\n\ts_nop 0\n\tglobal_load_lds_dwordx4 %0, %1" :: "v"(c.voff), "s"(src + (32 + c.wid) * 1024), "s"(c.ldsbase + (f & 3) * K4_SLOT + (32 + c.wid) * 1024) : "memory");
}
template <int N>
__device__ __forceinline__ void k4_xload(u32x4 (&xf)[36], const K4Ctx& c) {
    asm volatile("global_load_dwordx4 %0, %1, %2 offset:%3" : "=v"(xf[N]) : "v"(c.voffx), "s"(c.hsrc + (N / 4) * 4096), "i"(((N >> 1) & 1) * 2048 + (N & 1) * 256) : "memory");
}
template <int F, int S>
__device__ __forceinline__ void k4_steps(K4Acc& acc, const K4Acc& accP, u32x4 (&a)[K4_NB], u32x4 (&xf)[36], K4Epi& E, const K4Ctx& c) {
    if constexpr (S < 18) {
        constexpr int ig = (2 * S) % K4_NB, iu = (2 * S + 1) % K4_NB;
        auto refill = [&](auto idx, auto frag) {
            constexpr int i = decltype(idx)::value, n = decltype(frag)::value;
            if constexpr (n < 36) a[i] = lds128(c.slot[F & 3] + n * 1024);
            else if constexpr (F >= 2 && F < 8) a[i] = lds128(c.slot[(F + 1) & 3] + (n - 36) * 1024);
        };
        if constexpr (F == 0) {
            if constexpr (2 * S + K4_XD < 36) { k4_xload<2 * S + K4_XD>(xf, c); k4_xload<2 * S + K4_XD + 1>(xf, c); }
            constexpr int nx = (2 * S + K4_XD < 36) ? K4_XD : 34 - 2 * S;
            asm volatile("s_waitcnt vmcnt(%2)" : "+v"(xf[2 * S]), "+v"(xf[2 * S + 1]) : "i"(nx) : "memory");
        }
        acc.g[0] = mfma16(a[ig], xf[2 * S], S == 0 ? (f32x4){} : acc.g[0]); PIN(acc.g[0]);
        if constexpr (F > 0) { if constexpr (S == 0) k4_epi_begin(E, c, F - 1); k4_epi_slot<4 * S, 0>(E, accP, c); }
        SBAR();
        acc.g[1] = mfma16(a[ig], xf[2 * S + 1], S == 0 ? (f32x4){} : acc.g[1]); PIN(acc.g[1]);
        refill(std::integral_constant<int, ig>{}, std::integral_constant<int, 2 * S + K4_NB>{});
        if constexpr (F > 0) k4_epi_slot<4 * S + 1, 0>(E, accP, c);
        SBAR();
        acc.u[0] = mfma16(a[iu], xf[2 * S], S == 0 ? (f32x4){} : acc.u[0]); PIN(acc.u[0]);
        if constexpr (F == 0) { if constexpr (S % 3 == 0 && S / 3 < 5) k4_dma<S / 3>(c, 1); }
        else if constexpr (F == 1 || F == 2) { if constexpr (S < 10) k4_dma<S % 5>(c, 2 * F + S / 5); }
        else if constexpr (F <= 5) { if constexpr (S % 2 == 0 && S / 2 < 5) k4_dma<S / 2>(c, F + 3); }
        if constexpr (F > 0) k4_epi_slot<4 * S + 2, 0>(E, accP, c);
        SBAR();
        acc.u[1] = mfma16(a[iu], xf[2 * S + 1], S == 0 ? (f32x4){} : acc.u[1]); PIN(acc.u[1]);
        refill(std::integral_constant<int, iu>{}, std::integral_constant<int, 2 * S + 1 + K4_NB>{});
        if constexpr (F > 0) {
            k4_epi_slot<4 * S + 3, 0>(E, accP, c);
            if constexpr (S == 12) k4_store<0>(E, c, F - 1);
            if constexpr (S == 17) k4_store<1>(E, c, F - 1);
        }
        SBAR();
        k4_steps<F, S + 1>(acc, accP, a, xf, E, c);
    }
}
template <int M>
__device__ __forceinline__ void k4_epi_all(K4Epi& E, const K4Acc& acc, const K4Ctx& c) {
    if constexpr (M < 72) { k4_epi_slot<M, 0>(E, acc, c); k4_epi_all<M + 1>(E, acc, c); }
}

__global__ __launch_bounds__(512, 2) void k4_gateup(char* __restrict__ ws) {
    __shared__ __attribute__((aligned(16))) char smem[K4_CS + 288 * 4];
    const int tid = threadIdx.x, lane = tid & 63, wid = __builtin_amdgcn_readfirstlane(tid >> 6);
    const int bx = blockIdx.x, xcd = bx & 7, j = bx >> 3;
    const int mt = j & 7, fg = 4 * xcd + (j >> 3);
    const int tt = 8 * mt + wid, ft0 = 9 * fg;
    const int l15 = lane & 15, kq = lane >> 4;
    lptr lds = (lptr)smem;
    K4Ctx c;
    c.ldsbase = (unsigned)(uintptr_t)lds; c.wid = wid; c.voff = lane * 16; c.voffw = lane * 16 + wid * 4096;
    c.voffx = (kq >> 1) * 1024 + (kq & 1) * 512 + l15 * 16;
    c.voffa = (kq >> 1) * 512 + l15 * 16 + (kq & 1) * 8;
    c.wsrc = ws + OFF_WGU + (size_t)ft0 * K4_SLOT;
    c.hsrc = ws + OFF_HT + (size_t)tt * 36 * 1024;
    TS(4, 0); TR(4, 14);
    k4_dma<0>(c, 0); k4_dma<1>(c, 0); k4_dma<2>(c, 0); k4_dma<3>(c, 0); k4_dma<4>(c, 0);
    float csp[9];
    {
        const int cc = tid < 288 ? tid : 0;
        const float* p = (const float*)(ws + OFF_CSP) + (size_t)ft0 * 32 + cc;
#pragma unroll
        for (int i = 0; i < 9; ++i) csp[i] = p[(size_t)i * 288 * 32];
    }
    f32x2 stp[2][4];
#pragma unroll
    for (int u = 0; u < 2; ++u)
#pragma unroll
        for (int i = 0; i < 4; ++i) stp[u][i] = *(const f32x2*)(ws + OFF_STAT + (size_t)(i * 2048 + 32 * tt + 16 * u + l15) * 8);
    u32x4 xf[36];
    k4_xload<0>(xf, c); k4_xload<1>(xf, c); k4_xload<2>(xf, c); k4_xload<3>(xf, c);
    k4_xload<4>(xf, c); k4_xload<5>(xf, c); k4_xload<6>(xf, c); k4_xload<7>(xf, c);
    static_assert(K4_XD == 8, "");
    LAS float* csl = (LAS float*)(lds + K4_CS);
    {
        float s = 0.f;
#pragma unroll
        for (int i = 0; i < 9; ++i) s += csp[i];
        if (tid < 288) csl[tid] = s;
    }
#pragma unroll
    for (int u = 0; u < 2; ++u) {
        const f32x2 st = stp[u][0] + stp[u][1] + stp[u][2] + stp[u][3];
        c.m[u] = st[0] * (1.f / 576.f);
        c.V[u] = fmaxf(st[1] * (1.f / 576.f) - c.m[u] * c.m[u], 0.f) + 1e-5f;
        const float rstd = 1.0f / sqrtf(c.V[u]);
        c.C0[u] = -2.f * 0.7978845608028654f * 1.4426950408889634f * rstd; c.C1[u] = c.C0[u] * 0.044715f * rstd * rstd;
    }
    c.adst = ws + OFF_ACT + ((size_t)tt * 288 + ft0) * 1024;
    c.csw = csl + 4 * kq;
#pragma unroll
    for (int i = 0; i < 4; ++i) { c.slot[i] = lds + i * K4_SLOT + lane * 16; PIN(c.slot[i]); }
    u32x4 a[K4_NB];
    K4Acc accA, accB; K4Epi E;
#define K4_PRIME(F) _Pragma("unroll") for (int k = 0; k < K4_NB; ++k) a[k] = lds128(c.slot[(F) & 3] + k * 1024)
#define K4_BAR(N) asm volatile("s_waitcnt vmcnt(" #N ")\n\ts_barrier" ::: "memory")
    asm volatile("s_waitcnt vmcnt(%0) lgkmcnt(0)\n\ts_barrier" :: "i"(K4_XD) : "memory");
    TS(4, 1);
    K4_PRIME(0);
    k4_steps<0, 0>(accA, accB, a, xf, E, c);
    K4_BAR(0); TS(4, 2); K4_PRIME(1);
    k4_steps<1, 0>(accB, accA, a, xf, E, c);
    K4_BAR(2); K4_PRIME(2);
    k4_steps<2, 0>(accA, accB, a, xf, E, c);
    K4_BAR(6);
    k4_steps<3, 0>(accB, accA, a, xf, E, c);
    K4_BAR(6);
    k4_steps<4, 0>(accA, accB, a, xf, E, c);
    K4_BAR(6);
    k4_steps<5, 0>(accB, accA, a, xf, E, c);
    K4_BAR(6);
    k4_steps<6, 0>(accA, accB, a, xf, E, c);
    K4_BAR(4);
    k4_steps<7, 0>(accB, accA, a, xf, E, c);
    k4_steps<8, 0>(accA, accB, a, xf, E, c);
    TS(4, 10);
    k4_epi_begin(E, c, 8);
    k4_epi_all<6>(E, accA, c);
    k4_store<0>(E, c, 8); k4_store<1>(E, c, 8);
    TS(4, 11); TR(4, 15);
#undef K4_PRIME
#undef K4_BAR
}

constexpr int K5_SLOT = 20 * 1024, K5_NS = 6;

template <int N>
__device__ __forceinline__ void k5_steps(f32x4 (&acc)[4][6], const u32x4 (&B)[4], const u32x4 (&A)[6], u32x4 (&Bn)[4], u32x4 (&An)[6],
                                         lptr bp, lptr ap) {
    if constexpr (N < 24) {
        constexpr int i = N / 6, jx = N % 6;
        acc[i][jx] = mfma16(A[jx], B[i], acc[i][jx]); PIN(acc[i][jx]);
        if constexpr ((N & 1) == 0 && N / 2 < 10) {
            constexpr int r = N / 2;
            if constexpr (r == 0) Bn[0] = lds128(bp);
            else if constexpr (r <= 6) An[r - 1] = lds128(ap + (r - 1) * 1024);
            else Bn[r - 6] = lds128(bp + ((r - 6) >> 1) * 2048 + ((r - 6) & 1) * 256);
        }
        SBAR();
        k5_steps<N + 1>(acc, B, A, Bn, An, bp, ap);
    }
}

__global__ __launch_bounds__(512, 2) void k5_down(char* __restrict__ ws) {
    __shared__ __attribute__((aligned(16))) char smem[K5_NS * K5_SLOT];
    const int tid = threadIdx.x, lane = tid & 63, wid = __builtin_amdgcn_readfirstlane(tid >> 6);
    const int bx = blockIdx.x;
    const int idx = (bx & 7) * 30 + (bx >> 3);
    const int sp = idx / 48, rem = idx % 48, m4 = rem / 3, dg = rem % 3;
    const int k0 = sp * 29, nst = sp < 4 ? 29 : 28;
    lptr lds = (lptr)smem;
    const unsigned ldsbase = (unsigned)(uintptr_t)lds;
    TS(5, 0); TR(5, 14);
    if (wid >= 4) {
        const int l = wid - 4;
        const char* src[5]; int step[5];
#pragma unroll
        for (int i = 0; i < 5; ++i) {
            const bool isact = l < 2 && i < 4;
            const int wp = l < 2 ? l : 2 + 5 * (l - 2) + i;
            src[i] = isact ? ws + OFF_ACT + ((size_t)(4 * m4 + 2 * l + (i >> 1)) * 288 + 2 * k0 + (i & 1)) * 1024
                           : ws + OFF_WDT + ((size_t)(dg * 144 + k0) * 12 + wp) * 1024;
            step[i] = isact ? 2048 : 12288;
        }
        const int dst0 = l < 2 ? 4096 * l : 8192 + (2 + 5 * (l - 2)) * 1024;
        auto issue = [&](int s, int slot) {
#pragma unroll
            for (int i = 0; i < 5; ++i) {
                const int d = (l < 2 && i == 4) ? 8192 + l * 1024 : dst0 + i * 1024;
                dma16(lane * 16, src[i] + (size_t)s * step[i], ldsbase + slot * K5_SLOT + d);
            }
        };
        for (int s = 0; s < K5_NS - 1; ++s) issue(s, s);
        int slot = K5_NS - 1;
        for (int s = 0; s < nst; ++s) {
            const int issued = (s + K5_NS - 1 < nst ? s + K5_NS - 1 : nst);
            const int need = s + 2 < nst ? s + 2 : nst;
            const int fly = issued - need;
            if (fly >= 3) WAIT_VM(15); else if (fly == 2) WAIT_VM(10); else if (fly == 1) WAIT_VM(5); else WAIT_VM(0);
            __builtin_amdgcn_s_barrier();
            if (s + K5_NS - 1 < nst) { issue(s + K5_NS - 1, slot); }
            slot = slot == K5_NS - 1 ? 0 : slot + 1;
        }
        __builtin_amdgcn_s_barrier();
        return;
    }
    const int a = wid >> 1, b = wid & 1;
    const int l15 = lane & 15, kq = lane >> 4;
    lptr bp0 = lds + (2 * a) * 2048 + (kq >> 1) * 1024 + (kq & 1) * 512 + l15 * 16, ap0 = lds + 8192 + (6 * b) * 1024 + lane * 16;
    f32x4 acc[4][6] = {};
    u32x4 B0[4], A0[6], B1[4], A1[6];
    __builtin_amdgcn_s_barrier();
    asm volatile("" ::: "memory");
    TS(5, 1);
#pragma unroll
    for (int i = 0; i < 4; ++i) B0[i] = lds128(bp0 + (i >> 1) * 2048 + (i & 1) * 256);
#pragma unroll
    for (int i = 0; i < 6; ++i) A0[i] = lds128(ap0 + i * 1024);
    int slot = 0, s = 0;
#define K5_STAGE(BC, AC, BN, AN) do { \
        const int nslot = slot == K5_NS - 1 ? 0 : slot + 1; \
        k5_steps<0>(acc, BC, AC, BN, AN, bp0 + nslot * K5_SLOT, ap0 + nslot * K5_SLOT); \
        asm volatile("s_barrier" ::: "memory"); \
        slot = nslot; ++s; } while (0)
    for (; s + 1 < nst;) { K5_STAGE(B0, A0, B1, A1); K5_STAGE(B1, A1, B0, A0); }
    if (s < nst) K5_STAGE(B0, A0, B1, A1);
#undef K5_STAGE
    TS(5, 2);
    char* part = ws + OFF_PART + ((size_t)(sp * 48 + rem) * 4 + wid) * 24576 + lane * 16;
#pragma unroll
    for (int i = 0; i < 4; ++i)
#pragma unroll
        for (int jx = 0; jx < 6; ++jx) *(f32x4*)(part + (i * 6 + jx) * 1024) = acc[i][jx];
    TS(5, 3); TR(5, 15);
}

__global__ __launch_bounds__(256) void k6_reduce(const char* __restrict__ ws, u16* __restrict__ out) {
    const int lane = threadIdx.x & 63, gw = blockIdx.x * 4 + (threadIdx.x >> 6);
    const int rem = gw / 48, u = gw % 48, w = u / 12, tl = u % 12, i = tl / 3, p = tl % 3;
    const int m4 = rem / 3, dg = rem % 3, a = w >> 1, b = w & 1;
    const int tt = 4 * m4 + 2 * a + (i >> 1), uu = i & 1, dT = 6 * dg + 3 * b + p;
    const int l15 = lane & 15, q = lane >> 4;
    f32x4 v0 = {}, v1 = {};
#pragma unroll
    for (int sp = 0; sp < 5; ++sp) {
        const char* part = ws + OFF_PART + ((size_t)(sp * 48 + rem) * 4 + w) * 24576 + (i * 6 + 2 * p) * 1024 + lane * 16;
        v0 += *(const f32x4*)part; v1 += *(const f32x4*)(part + 1024);
    }
    const u32x4 h = *(const u32x4*)(ws + OFF_HT + (size_t)(tt * 36 + 2 * dT + (q >> 1)) * 1024 + (q & 1) * 512 + (16 * uu + l15) * 16);
    const u32x4 o = {pk2(v0[0] + bflo(h[0]), v0[1] + bfhi(h[0])), pk2(v0[2] + bflo(h[1]), v0[3] + bfhi(h[1])),
                     pk2(v1[0] + bflo(h[2]), v1[1] + bfhi(h[2])), pk2(v1[2] + bflo(h[3]), v1[3] + bfhi(h[3]))};
    *(u32x4*)(out + (size_t)(32 * tt + 16 * uu + l15) * D_ + 32 * dT + 8 * q) = o;
}

extern "C" void kernel_launch(void* const* d_in, const int* in_sizes, int n_in, void* d_out, int out_size, void* d_ws, size_t ws_size,
                              hipStream_t stream) {
    const u16* x = (const u16*)d_in[0];
    const u16* wqkv = (const u16*)d_in[1];
    const u16* wo = (const u16*)d_in[2];
    const u16* wg = (const u16*)d_in[4];
    const u16* wu = (const u16*)d_in[5];
    const u16* wd = (const u16*)d_in[6];
    char* ws = (char*)d_ws;
    k1_qkv<<<256, 512, 0, stream>>>(x, wqkv, ws);
    k2_attn<<<256, 512, 0, stream>>>(wo, wg, wu, wd, ws);
    k3_wo<<<256, 320, 0, stream>>>(x, ws);
    k4_gateup<<<256, 512, 0, stream>>>(ws);
    k5_down<<<240, 512, 0, stream>>>(ws);
    k6_reduce<<<576, 256, 0, stream>>>(ws, (u16*)d_out);
}
```

```cpp
#include <hip/hip_runtime.h>
#include <stdint.h>
#include <type_traits>

typedef unsigned short u16;
typedef __attribute__((ext_vector_type(8))) __bf16 bf16x8;
typedef __attribute__((ext_vector_type(2))) __bf16 bf16x2;
typedef __attribute__((ext_vector_type(4))) short s16x4;
typedef __attribute__((ext_vector_type(16))) float f32x16;
typedef __attribute__((ext_vector_type(4))) float f32x4;
typedef __attribute__((ext_vector_type(2))) float f32x2;
typedef __attribute__((ext_vector_type(4))) unsigned u32x4;
typedef __attribute__((ext_vector_type(2))) unsigned u32x2;
#define LAS __attribute__((address_space(3)))
typedef LAS char* lptr;

constexpr int S_ = 2048, D_ = 576, F_ = 4608;
constexpr int QTILE = 1152;
constexpr int KVPAIR = 4736, KVT = 2240, KVTAIL = 4480, KVHEAD = 32 * KVPAIR;
constexpr size_t OFF_QIMG = 0;
constexpr size_t OFF_KV   = OFF_QIMG + (size_t)32 * 64 * QTILE;
constexpr size_t OFF_OT   = OFF_KV + (size_t)32 * KVHEAD;
constexpr size_t OFF_HT   = OFF_OT + (size_t)64 * 36 * 1024;
constexpr size_t OFF_STAT = OFF_HT + (size_t)64 * 36 * 1024;
constexpr size_t OFF_WOT  = OFF_STAT + (size_t)4 * 2048 * 8;
constexpr size_t OFF_WGU  = OFF_WOT + (size_t)18 * 36 * 1024;
constexpr size_t OFF_CSP  = OFF_WGU + (size_t)288 * 36 * 1024;
constexpr size_t OFF_WDT  = OFF_CSP + (size_t)9 * 288 * 32 * 4;
constexpr size_t OFF_ACT  = OFF_WDT + (size_t)18 * 288 * 1024;
constexpr size_t OFF_PART = OFF_ACT + (size_t)64 * 288 * 1024;
constexpr size_t OFF_FLAG = OFF_PART + (size_t)5 * 128 * 18 * 1024;
constexpr int NFLAG = 2048;
constexpr int FL_PRE = 0, FL_A = 512, FL_B = 1024;
constexpr size_t OFF_END  = OFF_FLAG + NFLAG * 4;
constexpr size_t OFF_DBG  = (size_t)200 << 20;
#define TS(k, i)
#define TR(k, i)
#define TSL(v)
#define TSW(k, i, v)

__device__ __forceinline__ f32x16 mfma32(u32x4 a, u32x4 b, f32x16 c) {
    return __builtin_amdgcn_mfma_f32_32x32x16_bf16(__builtin_bit_cast(bf16x8, a), __builtin_bit_cast(bf16x8, b), c, 0, 0, 0);
}
__device__ __forceinline__ f32x16 mfma32k8(u32x2 a, u32x2 b, f32x16 c) {
    return __builtin_amdgcn_mfma_f32_32x32x8bf16_1k(__builtin_bit_cast(s16x4, a), __builtin_bit_cast(s16x4, b), c, 0, 0, 0);
}
__device__ __forceinline__ f32x4 mfma16(u32x4 a, u32x4 b, f32x4 c) {
    return __builtin_amdgcn_mfma_f32_16x16x32_bf16(__builtin_bit_cast(bf16x8, a), __builtin_bit_cast(bf16x8, b), c, 0, 0, 0);
}
__device__ __forceinline__ unsigned pk2(float lo, float hi) {
    f32x2 v = {lo, hi};
    return __builtin_bit_cast(unsigned, __builtin_convertvector(v, bf16x2));
}
__device__ __forceinline__ float bflo(unsigned u) { return __uint_as_float(u << 16); }
__device__ __forceinline__ float bfhi(unsigned u) { return __uint_as_float(u & 0xffff0000u); }
__device__ __forceinline__ void dma16(unsigned voff, const void* sbase, unsigned lds) {
    unsigned sv;
    asm volatile("s_mov_b32 %0, m0\n\ts_mov_b32 m0, %3\n\ts_nop 0\n\tglobal_load_lds_dwordx4 %1, %2\n\ts_mov_b32 m0, %0"
                 : "=&s"(sv) : "v"(voff), "s"(sbase), "s"(lds) : "memory");
}
__device__ __forceinline__ void dma4k_dev(unsigned voff, const void* sbase, unsigned lds) {
    asm volatile("s_mov_b32 m0, %2\n\ts_nop 0\n\tglobal_load_lds_dwordx4 %0, %1 sc1\n\tglobal_load_lds_dwordx4 %0, %1 offset:1024 sc1\n\t"
                 "global_load_lds_dwordx4 %0, %1 offset:2048 sc1\n\tglobal_load_lds_dwordx4 %0, %1 offset:3072 sc1"
                 :: "v"(voff), "s"(sbase), "s"(lds) : "memory");
}
__device__ __forceinline__ void dma1k_dev(unsigned voff, const void* sbase, unsigned lds) {
    asm volatile("s_mov_b32 m0, %2\n\ts_nop 0\n\tglobal_load_lds_dwordx4 %0, %1 sc1" :: "v"(voff), "s"(sbase), "s"(lds) : "memory");
}

__device__ __forceinline__ void st16(void* p, u32x4 v) { asm volatile("global_store_dwordx4 %0, %1, off sc1\n\ts_nop 1" :: "v"(p), "v"(v) : "memory"); }
__device__ __forceinline__ void st16f(void* p, f32x4 v) { st16(p, __builtin_bit_cast(u32x4, v)); }
__device__ __forceinline__ void st8(void* p, u32x2 v) { asm volatile("global_store_dwordx2 %0, %1, off sc1" :: "v"(p), "v"(v) : "memory"); }
__device__ __forceinline__ void st4(void* p, unsigned v) { asm volatile("global_store_dword %0, %1, off sc1" :: "v"(p), "v"(v) : "memory"); }

#define GAS __attribute__((address_space(1)))
__device__ __forceinline__ void flag_set(char* ws, int idx) {
    __hip_atomic_store((GAS unsigned*)(ws + OFF_FLAG) + idx, 1u, __ATOMIC_RELAXED, __HIP_MEMORY_SCOPE_AGENT);
}
__device__ __forceinline__ void flag_add(char* ws, int idx) {
    __hip_atomic_fetch_add((GAS unsigned*)(ws + OFF_FLAG) + idx, 1u, __ATOMIC_RELAXED, __HIP_MEMORY_SCOPE_AGENT);
}
__device__ __forceinline__ void flag_wait(char* ws, int idx, int stride, int n, unsigned want, int lane) {
    GAS unsigned* p = (GAS unsigned*)(ws + OFF_FLAG) + idx + (lane < n ? lane : 0) * stride;
    for (int spins = 0; spins < (1 << 18); ++spins) {
        const unsigned v = __hip_atomic_load(p, __ATOMIC_RELAXED, __HIP_MEMORY_SCOPE_AGENT);
        if (__all(v >= want)) break;
        __builtin_amdgcn_s_sleep(1);
    }
    asm volatile("" ::: "memory");
}
#define WAIT_VM(n) asm volatile("s_waitcnt vmcnt(" #n ")" ::: "memory")
#define WAIT_VM_BAR(n) asm volatile("s_waitcnt vmcnt(" #n ") lgkmcnt(0)\n\ts_barrier" ::: "memory")
#define SBAR() __builtin_amdgcn_sched_barrier(0)
__device__ __forceinline__ u32x4 lds128(lptr p) { return *(LAS const u32x4*)p; }
__device__ __forceinline__ u32x2 lds64(lptr p) { return *(LAS const u32x2*)p; }
typedef short v4i16_t __attribute__((ext_vector_type(4)));
__device__ __forceinline__ u32x2 ldstr(lptr p) { return __builtin_bit_cast(u32x2, __builtin_amdgcn_ds_read_tr16_b64_v4i16((LAS v4i16_t*)p)); }
__device__ __forceinline__ float xhalf(float v) {
    return __shfl_xor(v, 32);
}
__device__ __forceinline__ int crow(int r, int hh) { return (r & 3) + 8 * (r >> 2) + 4 * hh; }
__device__ __forceinline__ size_t kvtile(int h, int tt) { return (size_t)h * KVHEAD + (tt >> 1) * KVPAIR + (tt & 1) * KVT; }

constexpr int K1_BUF = 32768, K1_NB = 4, K1_ST = K1_NB * K1_BUF;
constexpr float QSCALE = 0.23570226039551584f * 1.4426950408889634f;
#define PIN(x) asm volatile("" : "+v"(x))

struct K1Frag { u32x4 x0, x1, w0, w1; };
template <bool V0, bool V1, bool T0, bool T1>
__device__ __forceinline__ void k1_body(char* __restrict__ ws, lptr lds, int mt, int g, int wid, int lane) {
    const int a = wid >> 1, b = wid & 1;
    const int hh = lane >> 5, l31 = lane & 31;
    const int t0 = 2 * b + a, t1 = 2 * b + (1 - a);
    const int f0 = 2 * a + b, f1 = 2 * a + (1 - b);
    const int sw = (l31 >> 1) & 7;
    const int xc = (hh ^ sw) << 4;
    int xa0[4], xa1[4];
#pragma unroll
    for (int s = 0; s < 4; ++s) { xa0[s] = (32 * t0 + l31) * 128 + (xc ^ (s << 5)); xa1[s] = (32 * t1 + l31) * 128 + (xc ^ (s << 5)); }
    const int q4 = (lane >> 2) & 3, p4 = lane & 3, cg = (lane >> 4) & 1;
    auto waddr = [&](int f) {
        const int c = f < 3 ? 4 * f + 2 * cg + (p4 >> 1) : 12 + p4;
        const int sub = f < 3 ? 8 * (p4 & 1) : 0;
        return 16384 + (8 * hh + q4) * 256 + ((c ^ (q4 << 2)) << 4) + sub;
    };
    const int wa0 = waddr(f0), wa1 = waddr(f1);
    const u32x4 ones = {0x3F803F80u, 0x3F803F80u, 0x3F803F80u, 0x3F803F80u};
    const bool onesrow = l31 >= 12;

    f32x16 acc00 = {}, acc01 = {}, acc10 = {}, acc11 = {}, accG = {};
    f32x4 accC = {};
    const unsigned pick = ((lane & 15) == ((lane >> 4) & 1)) ? 0x3F803F80u : 0u;
    const u32x4 sumA = {pick, pick, pick, pick};
    auto step = [&](K1Frag& C, K1Frag& N, lptr nb, int nks) {
        u32x2 lo, hi;
        if (T0) { for (int i = 0; i < 4; ++i) C.w0[i] = onesrow ? ones[i] : C.w0[i]; }
        if (T1) { for (int i = 0; i < 4; ++i) C.w1[i] = onesrow ? ones[i] : C.w1[i]; }
        acc00 = V0 ? mfma32(C.x0, C.w0, acc00) : mfma32(C.w0, C.x0, acc00); PIN(acc00); SBAR();
        N.x0 = lds128(nb + xa0[nks]); SBAR();
        accG = mfma32(C.x0, C.x0, accG); PIN(accG); SBAR();
        lo = ldstr(nb + wa0 + nks * 4096); SBAR();
        acc01 = V0 ? mfma32(C.x1, C.w0, acc01) : mfma32(C.w0, C.x1, acc01); PIN(acc01); SBAR();
        hi = ldstr(nb + wa0 + nks * 4096 + 1024); SBAR();
        acc10 = V1 ? mfma32(C.x0, C.w1, acc10) : mfma32(C.w1, C.x0, acc10); PIN(acc10); SBAR();
        N.x1 = lds128(nb + xa1[nks]); SBAR();
        accC = mfma16(sumA, C.w0, accC); PIN(accC); SBAR();
        N.w0 = (u32x4){lo[0], lo[1], hi[0], hi[1]};
        lo = ldstr(nb + wa1 + nks * 4096); SBAR();
        acc11 = V1 ? mfma32(C.x1, C.w1, acc11) : mfma32(C.w1, C.x1, acc11); PIN(acc11); SBAR();
        hi = ldstr(nb + wa1 + nks * 4096 + 1024);
        N.w1 = (u32x4){lo[0], lo[1], hi[0], hi[1]};
        SBAR();
    };
    K1Frag F0, F1, F2, F3;
    auto rdall = [&](K1Frag& N, int ks) {
        N.x0 = lds128(lds + xa0[ks]); N.x1 = lds128(lds + xa1[ks]);
        const u32x2 a0 = ldstr(lds + wa0 + ks * 4096), a1 = ldstr(lds + wa0 + ks * 4096 + 1024), b0 = ldstr(lds + wa1 + ks * 4096), b1 = ldstr(lds + wa1 + ks * 4096 + 1024);
        N.w0 = (u32x4){a0[0], a0[1], a1[0], a1[1]}; N.w1 = (u32x4){b0[0], b0[1], b1[0], b1[1]};
    };
    asm volatile("s_barrier" ::: "memory");
    rdall(F0, 0); rdall(F1, 1);
    for (int s = 0; s < 9; ++s) {
        lptr buf = lds + (s & 3) * K1_BUF, nbuf = lds + ((s + 1) & 3) * K1_BUF;
        step(F0, F2, buf, 2);
        step(F1, F3, buf, 3);
        asm volatile("s_barrier" ::: "memory");
        step(F2, F0, nbuf, 0);
        step(F3, F1, nbuf, 1);
    }
    TS(1, 3);
    LAS float* st = (LAS float*)(lds + K1_ST);
    LAS float* cs = (LAS float*)(lds + K1_ST + 1024);
    {
        float dg = accG[0];
        const int rsel = (l31 & 3) + 4 * (l31 >> 3);
#pragma unroll
        for (int r = 1; r < 16; ++r) dg = rsel == r ? accG[r] : dg;
        if (hh == ((l31 >> 2) & 1)) st[(32 * t0 + l31) * 2 + 1] = dg;
        if (lane < 16) { cs[32 * f0 + lane] = accC[0]; cs[32 * f0 + 16 + lane] = accC[1]; }
        auto rowsum = [&](const f32x16& acc, int t, bool isV) {
            if (!isV) { if (hh == 1) st[(32 * t + l31) * 2] = acc[4]; }
            else if (l31 == 12) {
#pragma unroll
                for (int r = 0; r < 16; ++r) st[(32 * t + crow(r, hh)) * 2] = acc[r];
            }
        };
        if (T0) { rowsum(acc00, t0, V0); rowsum(acc01, t1, V0); }
        if (T1) { rowsum(acc10, t0, V1); rowsum(acc11, t1, V1); }
    }
    asm volatile("s_waitcnt lgkmcnt(0)\n\ts_barrier" ::: "memory");
    {
        const f32x2 sv = *(LAS const f32x2*)(st + (32 * wid + l31) * 2);
        const float mean = sv[0] * (1.f / 576.f);
        const float var = fmaxf(sv[1] * (1.f / 576.f) - mean * mean, 0.f);
        const float rstd = __builtin_amdgcn_rsqf(var + 1e-5f);
        if (hh == 0) { st[512 + 32 * wid + l31] = rstd; st[512 + 128 + 32 * wid + l31] = mean * rstd; }
    }
    asm volatile("s_waitcnt lgkmcnt(0)\n\ts_barrier" ::: "memory");
    st += 512;
    TS(1, 4);
    char* qimg = ws + OFF_QIMG; char* kv = ws + OFF_KV;
    auto pair = [](const f32x16& a, int r) { return (f32x2){a[r], a[r + 1]}; };
    auto finish = [&](const f32x16& acc, int f, int t, bool isV) {
        const int tt = 4 * mt + t;
        unsigned pk[8];
        if (isV) {
            const float c = -cs[32 * f + l31];
#pragma unroll
            for (int q = 0; q < 4; ++q) {
                const f32x4 rs = *(LAS const f32x4*)(st + 32 * t + 8 * q + 4 * hh), mr = *(LAS const f32x4*)(st + 128 + 32 * t + 8 * q + 4 * hh);
                const f32x2 v0 = __builtin_elementwise_fma(pair(acc, 4 * q), (f32x2){rs[0], rs[1]}, (f32x2){mr[0], mr[1]} * (f32x2){c, c});
                const f32x2 v1 = __builtin_elementwise_fma(pair(acc, 4 * q + 2), (f32x2){rs[2], rs[3]}, (f32x2){mr[2], mr[3]} * (f32x2){c, c});
                pk[2 * q] = pk2(v0[0], v0[1]); pk[2 * q + 1] = pk2(v1[0], v1[1]);
            }
            int h, d;
            if (f < 3) { h = 2 * ((3 * g + f) & 15) + (l31 >> 4); d = l31 & 15; }
            else if (l31 < 12) { h = 2 * ((3 * g + (l31 >> 2)) & 15) + ((l31 >> 1) & 1); d = 16 + (l31 & 1); }
            else {
                const int i = l31 < 18 ? l31 - 12 : 0;
                h = 2 * ((3 * g + (i >> 1)) & 15) + (i & 1); d = 18;
#pragma unroll
                for (int i2 = 0; i2 < 8; ++i2) pk[i2] = 0x3F803F80u;
            }
            if (f < 3 || l31 < 18) {
                char* dst = kv + kvtile(h, tt) + 1024 + hh * 304 + d * 16;
                st16(dst, (u32x4){pk[0], pk[1], pk[2], pk[3]});
                st16(dst + 608, (u32x4){pk[4], pk[5], pk[6], pk[7]});
            }
            return;
        }
        const int P0 = 3 * g + (f < 3 ? f : 0), sec0 = P0 >> 4;
        const float sc = (f < 3 && sec0 == 0) ? QSCALE : 1.f;
        const float rs = st[32 * t + l31] * sc, nmr = -st[128 + 32 * t + l31] * sc;
        f32x2 v[8];
#pragma unroll
        for (int q = 0; q < 4; ++q) {
            const f32x4 cq = *(LAS const f32x4*)(cs + 32 * f + 8 * q + 4 * hh);
            v[2 * q] = __builtin_elementwise_fma(pair(acc, 4 * q), (f32x2){rs, rs}, (f32x2){cq[0], cq[1]} * (f32x2){nmr, nmr});
            v[2 * q + 1] = __builtin_elementwise_fma(pair(acc, 4 * q + 2), (f32x2){rs, rs}, (f32x2){cq[2], cq[3]} * (f32x2){nmr, nmr});
        }
        if (f < 3) {
            const int pp = P0 & 15;
#pragma unroll
            for (int i = 0; i < 8; ++i) pk[i] = pk2(v[i][0], v[i][1]);
            char* base = sec0 == 0 ? qimg + (size_t)(2 * pp * 64 + tt) * QTILE : kv + kvtile(2 * pp, tt);
            const size_t hs = sec0 == 0 ? (size_t)64 * QTILE : (size_t)KVHEAD;
            st16(base + lane * 16, (u32x4){pk[0], pk[1], pk[2], pk[3]});
            st16(base + hs + lane * 16, (u32x4){pk[4], pk[5], pk[6], pk[7]});
        } else {
#pragma unroll
            for (int u = 0; u < 2; ++u) {
                const int p = 2 * u + hh;
                if (p < 3) {
                    const int P = 3 * g + p, sec = P >> 4, pp = P & 15;
#pragma unroll
                    for (int o = 0; o < 2; ++o) {
                        const int h = 2 * pp + o;
                        const f32x2 e = v[2 * u + o];
                        if (sec == 0) {
                            st4(qimg + (size_t)(h * 64 + tt) * QTILE + 1024 + l31 * 4, pk2(e[0] * QSCALE, e[1] * QSCALE));
                        } else if (sec == 1) {
                            st4(kv + (size_t)h * KVHEAD + (tt >> 1) * KVPAIR + KVTAIL + l31 * 8 + (tt & 1) * 4, pk2(e[0], e[1]));
                        } else {
                            const unsigned pq = pk2(e[0], e[1]);
                            const int off = (2 * (l31 >> 4) + ((l31 >> 2) & 1)) * 304 + (((l31 >> 3) & 1) * 4 + (l31 & 3)) * 2;
                            char* dst = kv + kvtile(h, tt) + 1024 + off;
                            *(u16*)(dst + 16 * 16) = (u16)(pq & 0xffff);
                            *(u16*)(dst + 17 * 16) = (u16)(pq >> 16);
                            *(u16*)(dst + 18 * 16) = (u16)0x3F80;
                        }
                    }
                }
            }
        }
    };
    finish(acc00, f0, t0, V0); finish(acc01, f0, t1, V0); finish(acc10, f1, t0, V1); finish(acc11, f1, t1, V1);
    TS(1, 5); TR(1, 15);
}

__global__ __launch_bounds__(512, 2) void k1_qkv(const u16* __restrict__ x, const u16* __restrict__ w, char* __restrict__ ws) {
    __shared__ __attribute__((aligned(16))) char smem[K1_ST + 4096];
    const int lane = threadIdx.x & 63, wid = __builtin_amdgcn_readfirstlane(threadIdx.x >> 6);
    const int bx = blockIdx.x, xcd = bx & 7, j = bx >> 3;
    const int mt = (xcd >> 1) * 4 + (j & 3), g = (xcd & 1) * 8 + (j >> 2);
    lptr lds = (lptr)smem;
    TS(1, 0); TR(1, 14);
    if (bx < NFLAG / 512) ((unsigned*)(ws + OFF_FLAG))[bx * 512 + threadIdx.x] = 0u;
    if (wid >= 4) {
        const int l = wid - 4;
        const unsigned ldsbase = (unsigned)(uintptr_t)lds;
        unsigned xoff[4], woff[4];
#pragma unroll
        for (int i = 0; i < 4; ++i) {
            const int p = 4 * l + i;
            const int row = 8 * p + (lane >> 3), ch = (lane & 7) ^ ((row >> 1) & 7);
            xoff[i] = row * 1152 + ch * 16;
            const int kr = 4 * p + (lane >> 4), c = (lane & 15) ^ ((kr & 3) << 2);
            const int cs = c & 3;
            const int byte = c < 12 ? 72 * (c >> 2) + (cs < 2 ? 16 * cs : 40 + 16 * (cs - 2)) : 72 * ((c < 14 ? c : 14) - 12) + 32;
            woff[i] = kr * 3456 + byte;
        }
        const char* xb = (const char*)x + (size_t)mt * 128 * 1152;
        const char* wb = (const char*)w + g * 216;
#define K1_ROT 0
        const int rot = K1_ROT ? ((j >> 2) + 2 * (j & 3)) % 9 : 0;
        auto issue = [&](int s) {
            const unsigned dst = ldsbase + (s & 3) * K1_BUF + l * 4096;
            const int ks = s + rot < 9 ? s + rot : s + rot - 9;
#pragma unroll
            for (int i = 0; i < 4; ++i) {
                dma16(xoff[i], xb + ks * 128, dst + i * 1024);
                dma16(woff[i], wb + (size_t)ks * 64 * 3456, dst + 16384 + i * 1024);
            }
        };
        issue(0); issue(1);
        TS(1, 1);
        for (int s = 0; s < 9; ++s) {
            if (s < 8) WAIT_VM(8); else WAIT_VM(0);
            __builtin_amdgcn_s_barrier();
            if (s == 0) TS(1, 2);
            if (s < 7) issue(s + 2);
        }
        __builtin_amdgcn_s_barrier();
        __builtin_amdgcn_s_barrier(); __builtin_amdgcn_s_barrier();
        return;
    }
    const int a = wid >> 1, b = wid & 1;
    const int f0 = 2 * a + b;
    const bool vA = 3 * g >= 32, vB = 3 * g + 2 >= 32;
    if (a == 0) { if (vA) k1_body<true, true, false, false>(ws, lds, mt, g, wid, lane); else k1_body<false, false, false, false>(ws, lds, mt, g, wid, lane); }
    else if (f0 == 2) {
        if (vA) k1_body<true, true, false, true>(ws, lds, mt, g, wid, lane);
        else if (vB) k1_body<true, false, false, true>(ws, lds, mt, g, wid, lane);
        else k1_body<false, false, false, true>(ws, lds, mt, g, wid, lane);
    } else {
        if (vA) k1_body<true, true, true, false>(ws, lds, mt, g, wid, lane);
        else if (vB) k1_body<false, true, true, false>(ws, lds, mt, g, wid, lane);
        else k1_body<false, false, true, false>(ws, lds, mt, g, wid, lane);
    }
}

__device__ __forceinline__ int phi32(int rho) { const int r = (rho & 3) + 4 * (rho >> 3), hc = (rho >> 2) & 1; return 16 * (r >> 3) + 8 * hc + (r & 7); }
struct SideW { const u16 *wo, *wg, *wu, *wd; char* ws; };
struct SideJob {
    int type;
    const char* src[2];
    int pstep[4];
    unsigned voff;
    char* dst[2]; int dstep[4];
    int raddr[4];
    float* cs;
};
__device__ __forceinline__ SideJob side_job(const SideW& w, int item, int lane) {
    SideJob g;
    const int q4 = (lane >> 2) & 3, p4 = lane & 3, grp = lane >> 4;
    g.cs = nullptr; g.type = 3; g.voff = 0;
    int rs = D_ * 2;
    if (item < 1296) {
        const int U = item / 18, mat = (item % 18) / 9, sp = item % 9;
        g.type = 0; rs = F_ * 2;
        g.src[0] = (const char*)(mat ? w.wu : w.wg) + 128 * U + (size_t)(64 * sp) * rs; g.src[1] = g.src[0] + 32 * rs;
        g.dst[0] = w.ws + OFF_WGU + (size_t)((4 * U * 18 + 2 * sp) * 2 + mat) * 1024; g.dst[1] = g.dst[0] + 2048;
        g.cs = (float*)(w.ws + OFF_CSP) + (size_t)(sp * 288 + 4 * U) * 32 + mat * 16;
#pragma unroll
        for (int t = 0; t < 4; ++t) { g.pstep[t] = 8 * t * rs; g.dstep[t] = t * 36 * 1024; g.raddr[t] = (8 * grp + q4) * 128 + 32 * t + 8 * p4; }
        g.voff = (lane >> 3) * rs + (lane & 7) * 16;
    } else if (item < 1944) {
        const int it = item - 1296, V = it / 72, sp = it % 72;
        g.type = 1;
        g.src[0] = (const char*)w.wd + 128 * V + (size_t)(64 * sp) * rs; g.src[1] = g.src[0] + 32 * rs;
        g.dst[0] = w.ws + OFF_WDT + (size_t)(((V / 3) * 144 + 2 * sp) * 12 + (V % 3) * 4) * 1024; g.dst[1] = g.dst[0] + 12 * 1024;
#pragma unroll
        for (int t = 0; t < 4; ++t) { g.pstep[t] = 8 * t * rs; g.dstep[t] = t * 1024; g.raddr[t] = (8 * grp + q4) * 128 + 64 * (t >> 1) + 16 * p4 + 8 * (t & 1); }
        g.voff = (lane >> 3) * rs + (lane & 7) * 16;
    } else {
        const int it = item < 2025 ? item - 1944 : 0, V = it / 9, kq = it % 9;
        const bool mainp = kq < 8;
        const int jj = lane >> 3;
        if (item < 2025) g.type = 2;
#pragma unroll
        for (int b = 0; b < 2; ++b) {
            const int ks = 4 * kq + 2 * b;
            g.src[b] = (const char*)w.wo + 128 * V + (size_t)(mainp ? 18 * ks : 144 * (ks - 32)) * rs;
            g.dst[b] = w.ws + OFF_WOT + (size_t)(2 * V * 36 + ks) * 1024;
        }
#pragma unroll
        for (int t = 0; t < 4; ++t) {
            g.pstep[t] = (mainp ? 20 * (t >> 1) + 4 * (t & 1) : 144 * (t >> 1) + 72 * (t & 1)) * rs;
            g.dstep[t] = (t >> 1) * 1024 + (t & 1) * 36 * 1024;
            g.raddr[t] = (16 * (t >> 1) + 8 * (grp >> 1) + q4) * 128 + 64 * (t & 1) + 2 * phi32(16 * (grp & 1) + 4 * p4);
        }
        g.voff = (mainp ? 8 * (jj >> 2) + (jj & 3) : 18 * (jj >> 1) + (((jj >> 1) & 1) ? (jj & 1) : 16 + (jj & 1))) * rs + (lane & 7) * 16;
    }
    return g;
}
__device__ __forceinline__ void side_fetch(const SideJob& g, int b, unsigned ldsbuf) {
    if (g.type != 3) {
#pragma unroll
        for (int p = 0; p < 4; ++p) dma16(g.voff, g.src[b] + g.pstep[p], ldsbuf + p * 1024);
    }
}
__device__ __forceinline__ void side_finish(const SideJob& g, int b, lptr buf, f32x4 (&cs)[4], int lane) {
    if (g.type == 3) return;
    u32x4 v[4];
#pragma unroll
    for (int t = 0; t < 4; ++t) {
        const u32x2 lo = ldstr(buf + g.raddr[t]), hi = ldstr(buf + g.raddr[t] + 512);
        v[t] = (u32x4){lo[0], lo[1], hi[0], hi[1]};
    }
#pragma unroll
    for (int t = 0; t < 4; ++t) st16(g.dst[b] + g.dstep[t] + lane * 16, v[t]);
    if (g.type == 0) {
        const u32x4 ones = {0x3F803F80u, 0x3F803F80u, 0x3F803F80u, 0x3F803F80u};
#pragma unroll
        for (int t = 0; t < 4; ++t) cs[t] = mfma16(v[t], ones, cs[t]);
        if (b == 1 && (lane & 15) == 0) {
#pragma unroll
            for (int t = 0; t < 4; ++t) st16f(g.cs + t * 32 + (lane >> 4) * 4, cs[t]);
        }
    }
}

struct K2Regs { u32x4 km, v0, v1; u32x2 kt; float e0, e1; };
struct NoHook { __device__ __forceinline__ void operator()(int) const {} };
template <bool ODD, bool QKN, bool LDKN, bool LDVN, class H>
__device__ __forceinline__ void k2_step(f32x16& SC, f32x16& SN, f32x16& o, K2Regs& R, const u32x4& qm, const u32x2& qtA, const u32x2& qtB,
                                        lptr kp, lptr ktp, lptr vp, H&& hook) {
    float e[16]; unsigned pw[8];
#define EX(i) do { e[i] = __builtin_amdgcn_exp2f(SC[i]); PIN(e[i]); } while (0)
#define CV(i) do { pw[i] = pk2(e[2 * (i)], e[2 * (i) + 1]); PIN(pw[i]); } while (0)
    e[0] = R.e0; e[1] = R.e1;
    EX(2); EX(3); EX(4); EX(5);
    SBAR();
    if constexpr (QKN) { SN = mfma32k8(R.kt, ODD ? qtA : qtB, (f32x16){}); PIN(SN); SBAR(); }
    hook(0);
    EX(6); EX(7); CV(0); CV(1); CV(2); CV(3);
    SBAR();
    if constexpr (QKN) { SN = mfma32(R.km, qm, SN); PIN(SN); SBAR(); }
    if constexpr (LDKN) {
        R.km = lds128(kp + KVPAIR + (ODD ? KVT : 0));
        if constexpr (!ODD) R.kt = lds64(ktp + KVPAIR);
    }
    EX(8); EX(9); EX(10); EX(11);
    SBAR();
    asm volatile("" :: "v"(R.v0), "v"(R.v1));
    o = mfma32(R.v0, (u32x4){pw[0], pw[1], pw[2], pw[3]}, o); PIN(o); SBAR();
    hook(1);
    EX(12); EX(13); EX(14); EX(15); CV(4); CV(5); CV(6); CV(7);
    SBAR();
    if constexpr (QKN) { R.e0 = __builtin_amdgcn_exp2f(SN[0]); PIN(R.e0); R.e1 = __builtin_amdgcn_exp2f(SN[1]); PIN(R.e1); }
    else { asm volatile("s_nop 1"); }
    SBAR();
    o = mfma32(R.v1, (u32x4){pw[4], pw[5], pw[6], pw[7]}, o); PIN(o); SBAR();
    if constexpr (LDVN) { R.v0 = lds128(vp + (ODD ? KVPAIR : KVT)); R.v1 = lds128(vp + (ODD ? KVPAIR : KVT) + 608); }
    SBAR();
#undef EX
#undef CV
}

template <int K>
__device__ __forceinline__ void k3_steps(f32x16& acc, u32x4 (&af)[36], u32x4 (&b)[4], lptr bp) {
    if constexpr (K < 36) {
        asm volatile("s_waitcnt vmcnt(%1)" : "+v"(af[K]) : "i"(35 - K) : "memory");
        acc = mfma32(af[K], b[K & 3], K == 0 ? (f32x16){} : acc); PIN(acc);
        if constexpr (K + 4 < 36) b[K & 3] = lds128(bp + (K + 4) * 1024);
        SBAR();
        k3_steps<K + 1>(acc, af, b, bp);
    }
}
template <int A, int B>
__device__ __forceinline__ void k3_aloads(u32x4 (&af)[36], const char* wsrc, unsigned voff) {
    if constexpr (A < B) {
        asm volatile("global_load_dwordx4 %0, %1, %2 offset:%3 sc1" : "=v"(af[A]) : "v"(voff), "s"(wsrc + (A / 4) * 4096), "i"((A & 3) * 1024) : "memory");
        k3_aloads<A + 1, B>(af, wsrc, voff);
    }
}

constexpr int K4_SLOT = 36 * 1024, K4_CS = 4 * K4_SLOT, K4_NB = 4;
#define K4_XD 12

struct K4Acc { f32x4 g[2], u[2]; };
struct K4Epi { float g[8], u[8], e[8]; unsigned pk[4]; f32x4 cg, cu; };
constexpr int K4_NCH = 8, K4_NOP = 11;
struct K4Ctx {
    lptr slot[4]; LAS const float* csw; float m[2], C0[2], C1[2], V[2];
    const char* wsrc; unsigned ldsbase; int wid; unsigned voff, voffx, voffa; const char* hsrc; char* adst; bool young;
};
template <int R, int OP>
__device__ __forceinline__ void k4_epi_op(K4Epi& E, const K4Acc& acc, const K4Ctx& c) {
    constexpr int U = R >> 2, Q = R & 3;
    if constexpr (OP == 0) { E.g[R] = __builtin_fmaf(-c.m[U], E.cg[Q], acc.g[U][Q]); PIN(E.g[R]); }
    else if constexpr (OP == 1) { E.u[R] = __builtin_fmaf(-c.m[U], E.cu[Q], acc.u[U][Q]); PIN(E.u[R]); }
    else if constexpr (OP == 2) { E.e[R] = E.g[R] * E.g[R]; PIN(E.e[R]); }
    else if constexpr (OP == 3) { E.e[R] = __builtin_fmaf(E.e[R], c.C1[U], c.C0[U]); PIN(E.e[R]); }
    else if constexpr (OP == 4) { E.e[R] = E.g[R] * E.e[R]; PIN(E.e[R]); }
    else if constexpr (OP == 5) { E.e[R] = __builtin_amdgcn_exp2f(E.e[R]); PIN(E.e[R]); }
    else if constexpr (OP == 6) { E.e[R] = __builtin_fmaf(E.e[R], c.V[U], c.V[U]); PIN(E.e[R]); }
    else if constexpr (OP == 7) { E.e[R] = __builtin_amdgcn_rcpf(E.e[R]); PIN(E.e[R]); }
    else if constexpr (OP == 8) { E.u[R] = E.g[R] * E.u[R]; PIN(E.u[R]); }
    else if constexpr (OP == 9) { E.u[R] = E.u[R] * E.e[R]; PIN(E.u[R]); }
    else if constexpr (R & 1) { E.pk[R >> 1] = pk2(E.u[R - 1], E.u[R]); PIN(E.pk[R >> 1]); }
}
template <int M, int START, int CSP, int OSP, int R>
__device__ __forceinline__ void k4_epi_slot(K4Epi& E, const K4Acc& acc, const K4Ctx& c) {
    if constexpr (R < K4_NCH) {
        constexpr int d = M - (START + CSP * R);
        if constexpr (d >= 0 && d <= (K4_NOP - 1) * OSP && d % OSP == 0) k4_epi_op<R, d / OSP>(E, acc, c);
        k4_epi_slot<M, START, CSP, OSP, R + 1>(E, acc, c);
    }
}
__device__ __forceinline__ void k4_epi_begin(K4Epi& E, const K4Ctx& c, int f) {
    LAS const float* cs = c.csw + f * 32;
    E.cg = *(LAS const f32x4*)(cs); E.cu = *(LAS const f32x4*)(cs + 16);
}
template <int U>
__device__ __forceinline__ void k4_store(const K4Epi& E, const K4Ctx& c, int f) {
    st8(c.adst + (size_t)f * 1024 + U * 256 + c.voffa, (u32x2){E.pk[2 * U], E.pk[2 * U + 1]});
}
template <int I>
__device__ __forceinline__ void k4_dma(const K4Ctx& c, int f) {
    const char* src = c.wsrc + (size_t)f * K4_SLOT + c.wid * 4096;
    if constexpr (I == 0)
        asm volatile("s_mov_b32 m0, %2\n\ts_nop 0\n\tglobal_load_lds_dwordx4 %0, %1 sc1" :: "v"(c.voff), "s"(src), "s"(c.ldsbase + (f & 3) * K4_SLOT + c.wid * 4096) : "memory");
    else if constexpr (I < 4)
        asm volatile("global_load_lds_dwordx4 %0, %1 offset:%2 sc1" :: "v"(c.voff), "s"(src), "i"(I * 1024) : "memory");
    else if (c.wid < 4)
        dma1k_dev(c.voff, c.wsrc + (size_t)f * K4_SLOT + (32 + c.wid) * 1024, c.ldsbase + (f & 3) * K4_SLOT + (32 + c.wid) * 1024);
}
#define K4_SW 0
#define K4_SWA 0
template <int S, int SW>
__device__ __forceinline__ void k4_prio(const K4Ctx& c) {
    if constexpr (SW > 0 && S == 0) { if (c.young) __builtin_amdgcn_s_setprio(1); }
    if constexpr (SW > 0 && S == SW) { if (c.young) __builtin_amdgcn_s_setprio(0); }
}
template <int N>
__device__ __forceinline__ void k4_xload(u32x4 (&xf)[36], const K4Ctx& c) {
    asm volatile("global_load_dwordx4 %0, %1, %2 offset:%3 sc1" : "=v"(xf[N]) : "v"(c.voffx), "s"(c.hsrc + (N / 4) * 4096), "i"(((N >> 1) & 1) * 2048 + (N & 1) * 256) : "memory");
}
constexpr int k4_xissued(int s) {
    if (s < 0) return 0;
    int a = k4_xissued(s - 1) + 6, b = 2 * (s + 1) + K4_XD;
    a = a < b ? a : b;
    return a < 36 ? a : 36;
}
template <int A, int B>
__device__ __forceinline__ void k4_xloads(u32x4 (&xf)[36], const K4Ctx& c) {
    if constexpr (A < B) { k4_xload<A>(xf, c); k4_xloads<A + 1, B>(xf, c); }
}
template <int S, class ST>
__device__ __forceinline__ void k4_first(K4Acc& acc0, K4Acc& acc1, K4Acc& acc2, u32x4 (&a)[6], u32x4 (&xf)[36], const K4Ctx& c, ST&& stats) {
    if constexpr (S < 18) {
        k4_prio<S, K4_SWA>(c);
        constexpr int i0 = k4_xissued(S - 1), i1 = k4_xissued(S);
        k4_xloads<i0, i1>(xf, c);
        if constexpr (S == 0) stats();
        asm volatile("s_waitcnt vmcnt(%2)" : "+v"(xf[2 * S]), "+v"(xf[2 * S + 1]) : "i"(i1 - (2 * S + 2)) : "memory");
        auto pair = [&](f32x4& d0, f32x4& d1, auto idx) {
            constexpr int i = decltype(idx)::value;
            d0 = mfma16(a[i], xf[2 * S], S == 0 ? (f32x4){} : d0); PIN(d0); SBAR();
            d1 = mfma16(a[i], xf[2 * S + 1], S == 0 ? (f32x4){} : d1); PIN(d1);
            if constexpr (S < 17) a[i] = lds128(c.slot[i >> 1] + (2 * (S + 1) + (i & 1)) * 1024);
            SBAR();
        };
        pair(acc0.g[0], acc0.g[1], std::integral_constant<int, 0>{});
        pair(acc0.u[0], acc0.u[1], std::integral_constant<int, 1>{});
        if constexpr (S % 2 == 1 && S / 2 < 5) k4_dma<S / 2>(c, 3);
        pair(acc1.g[0], acc1.g[1], std::integral_constant<int, 2>{});
        pair(acc1.u[0], acc1.u[1], std::integral_constant<int, 3>{});
        pair(acc2.g[0], acc2.g[1], std::integral_constant<int, 4>{});
        pair(acc2.u[0], acc2.u[1], std::integral_constant<int, 5>{});
        k4_first<S + 1>(acc0, acc1, acc2, a, xf, c, stats);
    }
}
template <int F, int S>
__device__ __forceinline__ void k4_steps(K4Acc& acc, const K4Acc& accP, const K4Acc& accQ, u32x4 (&a)[K4_NB], u32x4 (&xf)[36], K4Epi& E, const K4Ctx& c) {
    if constexpr (S < 18) {
        constexpr int ig = (2 * S) % K4_NB, iu = (2 * S + 1) % K4_NB;
        auto refill = [&](auto idx, auto frag) {
            constexpr int i = decltype(idx)::value, n = decltype(frag)::value;
            if constexpr (n < 36) a[i] = lds128(c.slot[F & 3] + n * 1024);
            else if constexpr (F >= 4 && F < 8) a[i] = lds128(c.slot[(F + 1) & 3] + (n - 36) * 1024);
        };
        auto epi = [&](auto slot) {
            constexpr int M = decltype(slot)::value;
            if constexpr (F == 3) {
                if constexpr (M == 0) k4_epi_begin(E, c, 1);
                if constexpr (M <= 43) k4_epi_slot<M, 2, 3, 2, 0>(E, accP, c);
                if constexpr (M == 26) k4_epi_begin(E, c, 2);
                if constexpr (M >= 30) k4_epi_slot<M, 30, 3, 2, 0>(E, accQ, c);
                if constexpr (M == 33) k4_store<0>(E, c, 1);
                if constexpr (M == 44) k4_store<1>(E, c, 1);
                if constexpr (M == 61) k4_store<0>(E, c, 2);
                if constexpr (M == 71) k4_store<1>(E, c, 2);
            } else {
                if constexpr (M == 0) k4_epi_begin(E, c, F - 1);
                k4_epi_slot<M, 6, 6, 2, 0>(E, accP, c);
                if constexpr (M == 51) k4_store<0>(E, c, F - 1);
                if constexpr (M == 71) k4_store<1>(E, c, F - 1);
            }
        };
        k4_prio<S, K4_SW>(c);
        asm volatile("" :: "v"(a[ig]), "v"(a[iu]));
        acc.g[0] = mfma16(a[ig], xf[2 * S], S == 0 ? (f32x4){} : acc.g[0]); PIN(acc.g[0]);
        epi(std::integral_constant<int, 4 * S>{});
        SBAR();
        acc.g[1] = mfma16(a[ig], xf[2 * S + 1], S == 0 ? (f32x4){} : acc.g[1]); PIN(acc.g[1]);
        refill(std::integral_constant<int, ig>{}, std::integral_constant<int, 2 * S + K4_NB>{});
        epi(std::integral_constant<int, 4 * S + 1>{});
        SBAR();
        acc.u[0] = mfma16(a[iu], xf[2 * S], S == 0 ? (f32x4){} : acc.u[0]); PIN(acc.u[0]);
        if constexpr (F == 3 || F == 4) { if constexpr (S < 10) k4_dma<S % 5>(c, 2 * F - 2 + S / 5); }
        else if constexpr (F == 5) { if constexpr (S % 2 == 0 && S / 2 < 5) k4_dma<S / 2>(c, 8); }
        epi(std::integral_constant<int, 4 * S + 2>{});
        SBAR();
        acc.u[1] = mfma16(a[iu], xf[2 * S + 1], S == 0 ? (f32x4){} : acc.u[1]); PIN(acc.u[1]);
        refill(std::integral_constant<int, iu>{}, std::integral_constant<int, 2 * S + 1 + K4_NB>{});
        epi(std::integral_constant<int, 4 * S + 3>{});
        SBAR();
        k4_steps<F, S + 1>(acc, accP, accQ, a, xf, E, c);
    }
}
template <int M>
__device__ __forceinline__ void k4_epi_all(K4Epi& E, const K4Acc& acc, const K4Ctx& c) {
    if constexpr (M < 40) { k4_epi_slot<M, 0, 3, 1, 0>(E, acc, c); k4_epi_all<M + 1>(E, acc, c); }
}

constexpr int KB_OT = 3 * K4_SLOT, KB_RED = K4_CS + 1152, KB_CNT = KVHEAD, KB_STAT = KVHEAD + 64, KB_LDS = KB_STAT + 8192;
static_assert(KB_RED + 5 * 32 * 8 <= KB_CNT && KB_LDS <= 163840, "");

__global__ __launch_bounds__(512, 2) void kb_fused(const u16* __restrict__ x, const u16* __restrict__ wo, const u16* __restrict__ wg,
                                                   const u16* __restrict__ wu, const u16* __restrict__ wd, char* __restrict__ ws) {
    __shared__ __attribute__((aligned(16))) char smem[KB_LDS];
    lptr lds = (lptr)smem;
    const int bx = blockIdx.x, xcd = bx & 7, j = bx >> 3;
    const int member = 4 * xcd + (j >> 3), qb = j & 7;
    const int wid = __builtin_amdgcn_readfirstlane(threadIdx.x >> 6);
    const unsigned ldsbase = (unsigned)(uintptr_t)lds;
    {
    const int lane = threadIdx.x & 63;
    const int h = member, tt = 8 * qb + wid;
    const int hh = lane >> 5, l31 = lane & 31;
    TS(2, 0); TR(2, 14);
    const unsigned voff = lane * 16;
    const char* kvsrc = ws + OFF_KV + (size_t)h * KVHEAD;
    {
        const char* s0 = kvsrc + wid * 5120; const unsigned d0 = ldsbase + wid * 5120;
        asm volatile("s_mov_b32 m0, %2\n\ts_nop 0\n\tglobal_load_lds_dwordx4 %0, %1" :: "v"(voff), "s"(s0), "s"(d0) : "memory");
    }
    u32x4 qm; unsigned qt;
    {
        const char* qsrc = ws + OFF_QIMG + (size_t)(h * 64 + tt) * QTILE;
        const unsigned o2 = 1024 + l31 * 4;
        asm volatile("global_load_dwordx4 %0, %2, %4\n\tglobal_load_dword %1, %3, %4" : "=&v"(qm), "=&v"(qt) : "v"(voff), "v"(o2), "s"(qsrc) : "memory");
    }
    {
        const char* s0 = kvsrc + wid * 5120; const unsigned d0 = ldsbase + wid * 5120;
        asm volatile("global_load_lds_dwordx4 %0, %1 offset:1024\n\tglobal_load_lds_dwordx4 %0, %1 offset:2048\n\tglobal_load_lds_dwordx4 %0, %1 offset:3072"
                     :: "v"(voff), "s"(s0) : "memory");
        dma16(voff, s0 + 4096, d0 + 4096);
    }
    auto piece = [&](int c, int i) {
        int off = 40960 + 32768 * c + 4096 * wid;
        off = off + 4096 <= KVHEAD ? off : KVHEAD - 4096;
        if (i == 0) asm volatile("s_mov_b32 m0, %2\n\ts_nop 0\n\tglobal_load_lds_dwordx4 %0, %1" :: "v"(voff), "s"(kvsrc + off), "s"(ldsbase + off) : "memory");
        else if (i == 1) asm volatile("global_load_lds_dwordx4 %0, %1 offset:1024" :: "v"(voff), "s"(kvsrc + off) : "memory");
        else if (i == 2) asm volatile("global_load_lds_dwordx4 %0, %1 offset:2048" :: "v"(voff), "s"(kvsrc + off) : "memory");
        else asm volatile("global_load_lds_dwordx4 %0, %1 offset:3072" :: "v"(voff), "s"(kvsrc + off) : "memory");
    };
    lptr kp = lds + lane * 16, ktp = lds + KVTAIL + l31 * 8, vp = lds + 1024 + hh * 304 + l31 * 16;
    const bool young = wid >= 4;
#define PRIO(p) do { if (young) __builtin_amdgcn_s_setprio(p); } while (0)
    if (threadIdx.x == 0) *(LAS unsigned*)(lds + KB_CNT) = 0u;
    const SideW sw{wo, wg, wu, wd, ws};
    const int gw = bx * 8 + wid;
    const SideJob job = side_job(sw, gw, lane);
    asm volatile("s_waitcnt vmcnt(0) lgkmcnt(0)\n\ts_barrier" : "+v"(qm), "+v"(qt) :: "memory");
    TS(2, 1);
    PRIO(1);
    const u32x2 qtA = {hh == 0 ? qt : 0u, 0u}, qtB = {0u, hh == 0 ? qt : 0u};
    f32x16 o = {}, sA, sB;
    K2Regs R;
    R.km = lds128(kp); R.kt = lds64(ktp);
    sA = mfma32(R.km, qm, mfma32k8(R.kt, qtA, (f32x16){}));
    R.km = lds128(kp + KVT);
    R.v0 = lds128(vp); R.v1 = lds128(vp + 608);
    R.e0 = __builtin_amdgcn_exp2f(sA[0]); R.e1 = __builtin_amdgcn_exp2f(sA[1]);
#define TWO(HA, HB) do { \
        k2_step<false, true, true, true>(sA, sB, o, R, qm, qtA, qtB, kp, ktp, vp, HA); \
        k2_step<true, true, true, true>(sB, sA, o, R, qm, qtA, qtB, kp, ktp, vp, HB); \
        kp += KVPAIR; ktp += KVPAIR; vp += KVPAIR; } while (0)
    const NoHook nh;
#define TWO_DMA(c) TWO([&](int p) { piece(c, p); }, [&](int p) { piece(c, 2 + p); })
    TWO_DMA(0); TWO_DMA(1); TWO_DMA(2); TWO_DMA(3);
    TWO(nh, nh);
#define K2_SW0 10
    if (K2_SW0 == 10) PRIO(0);
    TWO(nh, nh);
    if (K2_SW0 == 12) PRIO(0);
    TWO(nh, nh);
    asm volatile("s_waitcnt vmcnt(0)\n\ts_barrier" ::: "memory");
    PRIO(1);
    TS(2, 2);
    const lptr sbuf = lds + wid * 4096;
    f32x4 cs[4] = {};
    side_fetch(job, 0, ldsbase + wid * 4096);
    int t = 14;
#define K2_S1 24
#define K2_S2 34
    for (; t < K2_S1; t += 2) TWO(nh, nh);
    WAIT_VM(0);
    side_finish(job, 0, sbuf, cs, lane);
    asm volatile("s_waitcnt lgkmcnt(0)" ::: "memory");
    side_fetch(job, 1, ldsbase + wid * 4096);
    for (; t < K2_S2; t += 2) TWO(nh, nh);
    asm volatile("s_waitcnt vmcnt(0)" ::: "memory");
    side_finish(job, 1, sbuf, cs, lane);
    TS(2, 3);
#define K2_SW 52
    for (; t < K2_S2 + 6; t += 2) TWO(nh, nh);
    WAIT_VM(0);
    if (lane == 0) {
        if (__hip_atomic_fetch_add((LAS unsigned*)(lds + KB_CNT), 1u, __ATOMIC_RELAXED, __HIP_MEMORY_SCOPE_WORKGROUP) == 7u) flag_add(ws, FL_PRE + (bx & 15) * 32);
    }
#pragma unroll 2
    for (; t < K2_SW; t += 2) TWO(nh, nh);
    PRIO(0);
    for (; t < 62; t += 2) TWO(nh, nh);
    k2_step<false, true, false, true>(sA, sB, o, R, qm, qtA, qtB, kp, ktp, vp, nh);
    k2_step<true, false, false, false>(sB, sA, o, R, qm, qtA, qtB, kp, ktp, vp, nh);
#undef TWO
#undef TWO_DMA
#undef PRIO
    TS(2, 4);
    float l = __shfl(o[10], l31);
    float fin = 0.f;
#pragma unroll
    for (int r = 0; r < 8; ++r) fin = __builtin_fmaf(o[r], 0.f, fin);
    if (hh == 0) fin = __builtin_fmaf(o[8], 0.f, __builtin_fmaf(o[9], 0.f, fin));
    const bool bad = !(l > 8.673617379884035e-19f && l < 1.152921504606847e18f) || fin != 0.f;
    if (__builtin_expect(__any(bad), 0)) {
        o = (f32x16){}; float m = -3.0e38f;
        const char* kq = kvsrc + lane * 16; const char* ktq = kvsrc + KVTAIL + l31 * 8; const char* vq = kvsrc + 1024 + hh * 304 + (l31 < 18 ? l31 : 18) * 16;
        for (int tb = 0; tb < 64; ++tb) {
            const int off = (tb >> 1) * KVPAIR, sub = (tb & 1) * KVT;
            const u32x4 km = *(const u32x4*)(kq + off + sub), v0 = *(const u32x4*)(vq + off + sub), v1 = *(const u32x4*)(vq + off + sub + 608);
            const u32x2 kt = *(const u32x2*)(ktq + off);
            const f32x16 s = mfma32(km, qm, mfma32k8(kt, (tb & 1) ? qtB : qtA, (f32x16){}));
            float mx = s[0];
#pragma unroll
            for (int r = 1; r < 16; ++r) mx = fmaxf(mx, s[r]);
            mx = fmaxf(mx, xhalf(mx));
            const float mn = fmaxf(m, mx), al = __builtin_amdgcn_exp2f(m - mn);
            m = mn;
            unsigned pw[8];
#pragma unroll
            for (int i = 0; i < 8; ++i) pw[i] = pk2(__builtin_amdgcn_exp2f(s[2 * i] - mn), __builtin_amdgcn_exp2f(s[2 * i + 1] - mn));
#pragma unroll
            for (int r = 0; r < 16; ++r) o[r] *= al;
            o = mfma32(v0, (u32x4){pw[0], pw[1], pw[2], pw[3]}, o);
            o = mfma32(v1, (u32x4){pw[4], pw[5], pw[6], pw[7]}, o);
        }
        l = __shfl(o[10], l31);
    }
    const float inv = 1.0f / l;
    char* ot = ws + OFF_OT + (size_t)tt * 36 * 1024;
    st16(ot + h * 1024 + lane * 16, (u32x4){pk2(o[0] * inv, o[1] * inv), pk2(o[2] * inv, o[3] * inv), pk2(o[4] * inv, o[5] * inv), pk2(o[6] * inv, o[7] * inv)});
    if (hh == 0) st4(ot + (32 + (h >> 3)) * 1024 + (((h & 7) >> 2) * 32 + l31) * 16 + (h & 3) * 4, pk2(o[8] * inv, o[9] * inv));
    TS(2, 5);
    }
    int tz = threadIdx.x; asm volatile("" : "+v"(tz));
    const int lane = tz & 63;
    const unsigned voff = lane * 16;
    K4Ctx c;
    c.ldsbase = ldsbase; c.wid = wid; c.voff = voff; c.young = wid >= 4;
    const int mtC = 2 * (xcd & 3) + (j & 1), ftC = 9 * (16 * (xcd >> 2) + (j >> 1));
    c.wsrc = ws + OFF_WGU + (size_t)ftC * K4_SLOT;
    {
        const int tt = 8 * qb + (member >> 2), cg = member & 3;
        const int dT0 = cg < 2 ? 5 * cg : 10 + 4 * (cg - 2), nT = cg < 2 ? 5 : 4;
        const int hh = lane >> 5, l31 = lane & 31;
        LAS f32x2* red = (LAS f32x2*)(lds + KB_RED);
        if (wid == 7) flag_wait(ws, FL_PRE, 32, 16, 16u, lane);
        WAIT_VM(0);
        if (wid < nT) {
            const int dT = dT0 + wid;
            const char* wsrc = ws + OFF_WOT + (size_t)dT * 36 * 1024;
            u32x4 af[36], xr0, xr1;
            asm volatile("s_barrier" ::: "memory");
            if (threadIdx.x == 0) flag_set(ws, FL_A + qb * 32 + member);
            k3_aloads<0, 36>(af, wsrc, voff);
            {
                const u16* xr = x + (size_t)(32 * tt + l31) * D_ + 32 * dT + 8 * hh;
                asm volatile("global_load_dwordx4 %0, %2, off\n\tglobal_load_dwordx4 %1, %2, off offset:32" : "=&v"(xr0), "=&v"(xr1) : "v"(xr) : "memory");
            }
            asm volatile("s_barrier" ::: "memory");
            TS(2, 6);
            u32x4 b[4];
            lptr bp = lds + KB_OT + lane * 16;
#pragma unroll
            for (int k = 0; k < 4; ++k) b[k] = lds128(bp + k * 1024);
            f32x16 acc;
            k3_steps<0>(acc, af, b, bp);
            asm volatile("s_waitcnt vmcnt(0)" : "+v"(xr0), "+v"(xr1) :: "memory");
            float s1 = 0.f, s2 = 0.f;
            unsigned hp[8];
#pragma unroll
            for (int i = 0; i < 8; ++i) {
                const unsigned xv = i < 4 ? xr0[i] : xr1[i - 4];
                hp[i] = pk2(acc[2 * i] + bflo(xv), acc[2 * i + 1] + bfhi(xv));
                const float a = bflo(hp[i]), bb = bfhi(hp[i]);
                s1 += a + bb; s2 += a * a + bb * bb;
            }
            char* ht = ws + OFF_HT + (size_t)(tt * 36 + 2 * dT) * 1024 + lane * 16;
            st16(ht, (u32x4){hp[0], hp[1], hp[2], hp[3]});
            st16(ht + 1024, (u32x4){hp[4], hp[5], hp[6], hp[7]});
            s1 += xhalf(s1); s2 += xhalf(s2);
            if (hh == 0) red[wid * 32 + l31] = (f32x2){s1, s2};
            asm volatile("s_waitcnt lgkmcnt(0)\n\ts_barrier" ::: "memory");
            if (threadIdx.x < 32) {
                f32x2 r = red[l31];
#pragma unroll
                for (int i = 1; i < 5; ++i) r += red[i * 32 + l31];
                st8(ws + OFF_STAT + (size_t)(cg * 2048 + 32 * tt + l31) * 8, __builtin_bit_cast(u32x2, r));
            }
        } else {
            asm volatile("s_barrier" ::: "memory");
            if (wid >= 5) {
                flag_wait(ws, FL_A + qb * 32, 1, 32, 1u, lane);
                const char* osrc = ws + OFF_OT + (size_t)tt * 36 * 1024 + (wid - 5) * 12288;
#pragma unroll
                for (int i = 0; i < 3; ++i) dma4k_dev(voff, osrc + i * 4096, ldsbase + KB_OT + (wid - 5) * 12288 + i * 4096);
                WAIT_VM(0);
            } else if (hh == 0) red[4 * 32 + l31] = (f32x2){0.f, 0.f};
            asm volatile("s_waitcnt lgkmcnt(0)\n\ts_barrier" ::: "memory");
            if (wid >= 5) {
                const int i0 = (wid - 5) * 9;
                float cp[2][9];
#pragma unroll
                for (int r = 0; r < 2; ++r) {
                    const int cc = 96 * (wid - 5) + 64 * r + lane;
                    const char* p = ws + OFF_CSP + (size_t)ftC * 128;
#pragma unroll
                    for (int i = 0; i < 9; ++i)
                        asm volatile("global_load_dword %0, %1, %2 sc1" : "=v"(cp[r][i]) : "v"((unsigned)(((r == 0 || lane < 32) ? cc : 0) * 4)), "s"(p + (size_t)i * 288 * 128) : "memory");
                }
#pragma unroll
                for (int i = 0; i < 9; ++i) dma4k_dev(voff, c.wsrc + (i0 + i) * 4096, ldsbase + (i0 + i) * 4096);
                asm volatile("s_waitcnt vmcnt(36)" : "+v"(cp[0][0]), "+v"(cp[0][1]), "+v"(cp[0][2]), "+v"(cp[0][3]), "+v"(cp[0][4]), "+v"(cp[0][5]), "+v"(cp[0][6]), "+v"(cp[0][7]), "+v"(cp[0][8]) :: "memory");
                asm volatile("" : "+v"(cp[1][0]), "+v"(cp[1][1]), "+v"(cp[1][2]), "+v"(cp[1][3]), "+v"(cp[1][4]), "+v"(cp[1][5]), "+v"(cp[1][6]), "+v"(cp[1][7]), "+v"(cp[1][8]));
                LAS float* csl = (LAS float*)(lds + K4_CS) + 96 * (wid - 5);
                float s0 = 0.f, s1 = 0.f;
#pragma unroll
                for (int i = 0; i < 9; ++i) { s0 += cp[0][i]; s1 += cp[1][i]; }
                csl[lane] = s0;
                if (lane < 32) csl[64 + lane] = s1;
            }
            asm volatile("s_barrier" ::: "memory");
        }
        asm volatile("s_waitcnt vmcnt(0) lgkmcnt(0)\n\ts_barrier" ::: "memory");
        TS(2, 7);
        if (wid == 7) {
            if (lane == 0) flag_set(ws, FL_B + qb * 32 + member);
            flag_wait(ws, FL_B + mtC * 32, 1, 32, 1u, lane);
        }
        asm volatile("s_barrier" ::: "memory");
    }
    TS(2, 8);
    const int tt = 8 * mtC + wid;
    const int l15 = lane & 15, kq = lane >> 4;
    c.voffx = (kq >> 1) * 1024 + (kq & 1) * 512 + l15 * 16;
    c.voffa = (kq >> 1) * 512 + l15 * 16 + (kq & 1) * 8;
    c.hsrc = ws + OFF_HT + (size_t)tt * 36 * 1024;
    u32x4 xf[36];
    auto stats = [&]() { dma1k_dev(voff, ws + OFF_STAT + (size_t)((wid >> 1) * 2048 + 256 * mtC + 128 * (wid & 1)) * 8, ldsbase + KB_STAT + wid * 1024); };
    c.adst = ws + OFF_ACT + ((size_t)tt * 288 + ftC) * 1024;
    c.csw = (LAS const float*)(lds + K4_CS) + 4 * kq;
#pragma unroll
    for (int i = 0; i < 4; ++i) { c.slot[i] = lds + i * K4_SLOT + lane * 16; PIN(c.slot[i]); }
    K4Acc accA, accB, accC; K4Epi E;
#define K4_BAR(N) asm volatile("s_waitcnt vmcnt(" #N ")\n\ts_barrier" ::: "memory")
    {
        u32x4 a6[6];
#pragma unroll
        for (int i = 0; i < 6; ++i) a6[i] = lds128(c.slot[i >> 1] + (i & 1) * 1024);
        k4_first<0>(accA, accB, accC, a6, xf, c, stats);
    }
    K4_BAR(0);
    TS(2, 9);
    u32x4 a[K4_NB];
#define K4_PRIME(F) _Pragma("unroll") for (int k = 0; k < K4_NB; ++k) a[k] = lds128(c.slot[(F) & 3] + k * 1024)
    K4_PRIME(3);
#pragma unroll
    for (int u = 0; u < 2; ++u) {
        LAS const f32x2* sp = (LAS const f32x2*)(lds + KB_STAT) + 32 * wid + 16 * u + l15;
        const f32x2 st = sp[0] + sp[256] + sp[512] + sp[768];
        c.m[u] = st[0] * (1.f / 576.f);
        c.V[u] = fmaxf(st[1] * (1.f / 576.f) - c.m[u] * c.m[u], 0.f) + 1e-5f;
        const float rstd = __builtin_amdgcn_rsqf(c.V[u]);
        c.C0[u] = -2.f * 0.7978845608028654f * 1.4426950408889634f * rstd; c.C1[u] = c.C0[u] * 0.044715f * rstd * rstd;
    }
    k4_epi_begin(E, c, 0);
    k4_epi_all<0>(E, accA, c);
    k4_store<0>(E, c, 0); k4_store<1>(E, c, 0);
    SBAR();
    k4_steps<3, 0>(accA, accB, accC, a, xf, E, c);
    K4_BAR(3); K4_PRIME(4);
    k4_steps<4, 0>(accB, accA, accA, a, xf, E, c);
    K4_BAR(6);
    k4_steps<5, 0>(accA, accB, accB, a, xf, E, c);
    K4_BAR(6);
    k4_steps<6, 0>(accB, accA, accA, a, xf, E, c);
    K4_BAR(4);
    k4_steps<7, 0>(accA, accB, accB, a, xf, E, c);
    k4_steps<8, 0>(accB, accA, accA, a, xf, E, c);
    TS(2, 10);
    k4_epi_begin(E, c, 8);
    k4_epi_all<0>(E, accB, c);
    k4_store<0>(E, c, 8); k4_store<1>(E, c, 8);
    TS(2, 11); TR(2, 15);
#undef K4_BAR
#undef K4_PRIME
}

constexpr int K5_STEP = 20 * 1024, K5_SLOT = 2 * K5_STEP, K5_NS = 4;

template <int N>
__device__ __forceinline__ void k5_steps(f32x4 (&acc)[4][6], const u32x4 (&B)[4], const u32x4 (&A)[6], u32x4 (&Bn)[4], u32x4 (&An)[6],
                                         lptr bp, lptr ap) {
    if constexpr (N < 24) {
        constexpr int i = N / 6, jx = N % 6;
        acc[i][jx] = mfma16(A[jx], B[i], acc[i][jx]); PIN(acc[i][jx]);
        if constexpr ((N & 1) == 0 && N / 2 < 10) {
            constexpr int r = N / 2;
            if constexpr (r == 0) Bn[0] = lds128(bp);
            else if constexpr (r <= 6) An[r - 1] = lds128(ap + (r - 1) * 1024);
            else Bn[r - 6] = lds128(bp + ((r - 6) >> 1) * 2048 + ((r - 6) & 1) * 256);
        }
        SBAR();
        k5_steps<N + 1>(acc, B, A, Bn, An, bp, ap);
    }
}

__global__ __launch_bounds__(512, 2) void k5_down(char* __restrict__ ws) {
    __shared__ __attribute__((aligned(16))) char smem[K5_NS * K5_SLOT];
    const int tid = threadIdx.x, lane = tid & 63, wid = __builtin_amdgcn_readfirstlane(tid >> 6);
    const int bx = blockIdx.x;
    const int idx = (bx & 7) * 30 + (bx >> 3);
    const int sp = idx / 48, rem = idx % 48, m4 = rem / 3, dg = rem % 3;
    const int k0 = sp < 2 ? 30 * sp : 60 + 28 * (sp - 2), nst = sp < 2 ? 15 : 14;
    lptr lds = (lptr)smem;
    const unsigned ldsbase = (unsigned)(uintptr_t)lds;
    TS(5, 0); TR(5, 14);
    if (wid >= 4) {
        const int l = wid - 4;
        const unsigned voff = lane * 16;
        const char* asrc = ws + OFF_ACT + ((size_t)(4 * m4 + l) * 288 + 2 * k0) * 1024;
        const char* wsrc = ws + OFF_WDT + ((size_t)(dg * 144 + k0) * 12 + 3 * l) * 1024;
        const unsigned adst = ldsbase + l * 2048, wdst = ldsbase + 8192 + l * 3072;
        auto issue1 = [&](int ks, unsigned dst) {
            asm volatile("s_mov_b32 m0, %2\n\ts_nop 0\n\tglobal_load_lds_dwordx4 %0, %1\n\tglobal_load_lds_dwordx4 %0, %1 offset:1024"
                         :: "v"(voff), "s"(asrc + (size_t)ks * 2048), "s"(adst + dst) : "memory");
            asm volatile("s_mov_b32 m0, %2\n\ts_nop 0\n\tglobal_load_lds_dwordx4 %0, %1\n\tglobal_load_lds_dwordx4 %0, %1 offset:1024\n\tglobal_load_lds_dwordx4 %0, %1 offset:2048"
                         :: "v"(voff), "s"(wsrc + (size_t)ks * 12288), "s"(wdst + dst) : "memory");
        };
        auto issue = [&](int s, int slot) { issue1(2 * s, slot * K5_SLOT); issue1(2 * s + 1, slot * K5_SLOT + K5_STEP); };
        issue(0, 0); issue1(2, K5_SLOT);
        WAIT_VM(5); __builtin_amdgcn_s_barrier();
        issue1(3, K5_SLOT + K5_STEP); issue(2, 2);
        int slot = 3;
        for (int s = 1; s <= nst; ++s) {
            if (s + 1 < nst) WAIT_VM(10); else WAIT_VM(0);
            __builtin_amdgcn_s_barrier();
            if (s + 2 < nst) issue(s + 2, slot);
            slot = (slot + 1) & 3;
        }
        return;
    }
    const int a = wid >> 1, b = wid & 1;
    const int l15 = lane & 15, kq = lane >> 4;
    lptr bp0 = lds + (2 * a) * 2048 + (kq >> 1) * 1024 + (kq & 1) * 512 + l15 * 16, ap0 = lds + 8192 + (6 * b) * 1024 + lane * 16;
    f32x4 acc[4][6] = {};
    u32x4 B0[4], A0[6], B1[4], A1[6];
    __builtin_amdgcn_s_barrier();
    asm volatile("" ::: "memory");
    TS(5, 1);
#pragma unroll
    for (int i = 0; i < 4; ++i) B0[i] = lds128(bp0 + (i >> 1) * 2048 + (i & 1) * 256);
#pragma unroll
    for (int i = 0; i < 6; ++i) A0[i] = lds128(ap0 + i * 1024);
    int slot = 0;
    for (int s = 0; s < nst; ++s) {
        const int nslot = (slot + 1) & 3;
        k5_steps<0>(acc, B0, A0, B1, A1, bp0 + slot * K5_SLOT + K5_STEP, ap0 + slot * K5_SLOT + K5_STEP);
        asm volatile("s_barrier" ::: "memory");
        k5_steps<0>(acc, B1, A1, B0, A0, bp0 + nslot * K5_SLOT, ap0 + nslot * K5_SLOT);
        slot = nslot;
    }
    TS(5, 2);
    {
        char* pbase = ws + OFF_PART + (size_t)sp * (128 * 18 * 1024) + lane * 16;
#pragma unroll
        for (int n = 0; n < 12; ++n) {
            const int i = n / 3, p = n % 3;
            const int t16 = 2 * (4 * m4 + 2 * a + (i >> 1)) + (i & 1), dT = 6 * dg + 3 * b + p;
            const f32x4 v0 = acc[i][2 * p], v1 = acc[i][2 * p + 1];
            st16(pbase + (size_t)(t16 * 18 + dT) * 1024, (u32x4){pk2(v0[0], v0[1]), pk2(v0[2], v0[3]), pk2(v1[0], v1[1]), pk2(v1[2], v1[3])});
        }
    }
    TS(5, 3); TR(5, 15);
}
__global__ __launch_bounds__(256) void k6_reduce(const char* __restrict__ ws, u16* __restrict__ out) {
    const int lane = threadIdx.x & 63, gw = blockIdx.x * 4 + (threadIdx.x >> 6);
    const int t16 = gw / 18, dT = gw % 18, l15 = lane & 15, q = lane >> 4;
    u32x4 w[5];
#pragma unroll
    for (int sp = 0; sp < 5; ++sp) w[sp] = *(const u32x4*)(ws + OFF_PART + (size_t)sp * (128 * 18 * 1024) + (size_t)gw * 1024 + lane * 16);
    const u32x4 h = *(const u32x4*)(ws + OFF_HT + (size_t)((t16 >> 1) * 36 + 2 * dT + (q >> 1)) * 1024 + (q & 1) * 512 + (16 * (t16 & 1) + l15) * 16);
    u32x4 o;
#pragma unroll
    for (int i = 0; i < 4; ++i) {
        float lo = bflo(h[i]), hi = bfhi(h[i]);
        float slo = 0.f, shi = 0.f;
#pragma unroll
        for (int sp = 0; sp < 5; ++sp) { slo += bflo(w[sp][i]); shi += bfhi(w[sp][i]); }
        o[i] = pk2(slo + lo, shi + hi);
    }
    *(u32x4*)(out + (size_t)(16 * t16 + l15) * D_ + 32 * dT + 8 * q) = o;
}

static void launch_one(int k, void* const* d_in, void* d_out, void* d_ws, hipStream_t stream) {
    const u16* x = (const u16*)d_in[0];
    const u16* wqkv = (const u16*)d_in[1];
    const u16* wo = (const u16*)d_in[2];
    const u16* wg = (const u16*)d_in[4];
    const u16* wu = (const u16*)d_in[5];
    const u16* wd = (const u16*)d_in[6];
    char* ws = (char*)d_ws;
    switch (k) {
    case 1: k1_qkv<<<256, 512, 0, stream>>>(x, wqkv, ws); break;
    case 2: kb_fused<<<256, 512, 0, stream>>>(x, wo, wg, wu, wd, ws); break;
    case 3: k5_down<<<240, 512, 0, stream>>>(ws); break;
    case 4: k6_reduce<<<576, 256, 0, stream>>>(ws, (u16*)d_out); break;
    }
}
extern "C" void kernel_launch(void* const* d_in, const int* in_sizes, int n_in, void* d_out, int out_size, void* d_ws, size_t ws_size,
                              hipStream_t stream) {
    for (int k = 1; k <= 4; ++k) launch_one(k, d_in, d_out, d_ws, stream);
}
```

```cpp
#include <hip/hip_runtime.h>
#include <stdint.h>
#include <type_traits>

typedef unsigned short u16;
typedef __attribute__((ext_vector_type(8))) __bf16 bf16x8;
typedef __attribute__((ext_vector_type(2))) __bf16 bf16x2;
typedef __attribute__((ext_vector_type(4))) short s16x4;
typedef __attribute__((ext_vector_type(16))) float f32x16;
typedef __attribute__((ext_vector_type(4))) float f32x4;
typedef __attribute__((ext_vector_type(2))) float f32x2;
typedef __attribute__((ext_vector_type(4))) unsigned u32x4;
typedef __attribute__((ext_vector_type(2))) unsigned u32x2;
#define LAS __attribute__((address_space(3)))
typedef LAS char* lptr;

constexpr int S_ = 2048, D_ = 576, F_ = 4608;
constexpr int QTILE = 1152;
constexpr int KVPAIR = 4736, KVT = 2240, KVTAIL = 4480, KVHEAD = 32 * KVPAIR;
constexpr size_t OFF_QIMG = 0;
constexpr size_t OFF_KV   = OFF_QIMG + (size_t)32 * 64 * QTILE;
constexpr size_t OFF_OT   = OFF_KV + (size_t)32 * KVHEAD;
constexpr size_t OFF_HT   = OFF_OT + (size_t)64 * 36 * 1024;
constexpr size_t OFF_STAT = OFF_HT + (size_t)64 * 36 * 1024;
constexpr size_t OFF_WOT  = OFF_STAT + (size_t)4 * 2048 * 8;
constexpr size_t OFF_WGU  = OFF_WOT + (size_t)18 * 36 * 1024;
constexpr size_t OFF_CSP  = OFF_WGU + (size_t)288 * 36 * 1024;
constexpr size_t OFF_WDT  = OFF_CSP + (size_t)9 * 288 * 32 * 4;
constexpr size_t OFF_ACT  = OFF_WDT + (size_t)18 * 288 * 1024;
constexpr size_t OFF_PART = OFF_ACT + (size_t)64 * 288 * 1024;
constexpr size_t OFF_FLAG = OFF_PART + (size_t)5 * 128 * 18 * 1024;
constexpr int NFLAG = 3072;
constexpr int FL_PRE = 0, FL_B = 512, FL_A = 1024;
constexpr size_t OFF_END  = OFF_FLAG + NFLAG * 4;
constexpr size_t OFF_DBG  = (size_t)200 << 20;
#define TS(k, i)
#define TR(k, i)
#define TSL(v)
#define TSW(k, i, v)

__device__ __forceinline__ f32x16 mfma32(u32x4 a, u32x4 b, f32x16 c) {
    return __builtin_amdgcn_mfma_f32_32x32x16_bf16(__builtin_bit_cast(bf16x8, a), __builtin_bit_cast(bf16x8, b), c, 0, 0, 0);
}
__device__ __forceinline__ f32x16 mfma32k8(u32x2 a, u32x2 b, f32x16 c) {
    return __builtin_amdgcn_mfma_f32_32x32x8bf16_1k(__builtin_bit_cast(s16x4, a), __builtin_bit_cast(s16x4, b), c, 0, 0, 0);
}
__device__ __forceinline__ f32x4 mfma16(u32x4 a, u32x4 b, f32x4 c) {
    return __builtin_amdgcn_mfma_f32_16x16x32_bf16(__builtin_bit_cast(bf16x8, a), __builtin_bit_cast(bf16x8, b), c, 0, 0, 0);
}
__device__ __forceinline__ unsigned pk2(float lo, float hi) {
    f32x2 v = {lo, hi};
    return __builtin_bit_cast(unsigned, __builtin_convertvector(v, bf16x2));
}
__device__ __forceinline__ float bflo(unsigned u) { return __uint_as_float(u << 16); }
__device__ __forceinline__ float bfhi(unsigned u) { return __uint_as_float(u & 0xffff0000u); }
__device__ __forceinline__ void dma16(unsigned voff, const void* sbase, unsigned lds) {
    unsigned sv;
    asm volatile("s_mov_b32 %0, m0\n\ts_mov_b32 m0, %3\n\ts_nop 0\n\tglobal_load_lds_dwordx4 %1, %2\n\ts_mov_b32 m0, %0"
                 : "=&s"(sv) : "v"(voff), "s"(sbase), "s"(lds) : "memory");
}
__device__ __forceinline__ void dma4k_dev(unsigned voff, const void* sbase, unsigned lds) {
    asm volatile("s_mov_b32 m0, %2\n\ts_nop 0\n\tglobal_load_lds_dwordx4 %0, %1 sc1\n\tglobal_load_lds_dwordx4 %0, %1 offset:1024 sc1\n\t"
                 "global_load_lds_dwordx4 %0, %1 offset:2048 sc1\n\tglobal_load_lds_dwordx4 %0, %1 offset:3072 sc1"
                 :: "v"(voff), "s"(sbase), "s"(lds) : "memory");
}
__device__ __forceinline__ void dma1k_dev(unsigned voff, const void* sbase, unsigned lds) {
    asm volatile("s_mov_b32 m0, %2\n\ts_nop 0\n\tglobal_load_lds_dwordx4 %0, %1 sc1" :: "v"(voff), "s"(sbase), "s"(lds) : "memory");
}

__device__ __forceinline__ void st16(void* p, u32x4 v) { asm volatile("global_store_dwordx4 %0, %1, off sc1\n\ts_nop 1" :: "v"(p), "v"(v) : "memory"); }
__device__ __forceinline__ void st16f(void* p, f32x4 v) { st16(p, __builtin_bit_cast(u32x4, v)); }
__device__ __forceinline__ void st8(void* p, u32x2 v) { asm volatile("global_store_dwordx2 %0, %1, off sc1" :: "v"(p), "v"(v) : "memory"); }
__device__ __forceinline__ void st4(void* p, unsigned v) { asm volatile("global_store_dword %0, %1, off sc1" :: "v"(p), "v"(v) : "memory"); }

#define GAS __attribute__((address_space(1)))
__device__ __forceinline__ void flag_set(char* ws, int idx) {
    __hip_atomic_store((GAS unsigned*)(ws + OFF_FLAG) + idx, 1u, __ATOMIC_RELAXED, __HIP_MEMORY_SCOPE_AGENT);
}
__device__ __forceinline__ void flag_add(char* ws, int idx) {
    __hip_atomic_fetch_add((GAS unsigned*)(ws + OFF_FLAG) + idx, 1u, __ATOMIC_RELAXED, __HIP_MEMORY_SCOPE_AGENT);
}
__device__ __forceinline__ void flag_wait(char* ws, int idx, unsigned want) {
    GAS unsigned* p = (GAS unsigned*)(ws + OFF_FLAG) + idx;
    for (int spins = 0; spins < (1 << 18); ++spins) {
        const unsigned v = __hip_atomic_load(p, __ATOMIC_RELAXED, __HIP_MEMORY_SCOPE_AGENT);
        if (__all(v >= want)) break;
        __builtin_amdgcn_s_sleep(1);
    }
    asm volatile("" ::: "memory");
}
__device__ __forceinline__ void lds_wait(__attribute__((address_space(3))) char* p) {
    for (int spins = 0; spins < (1 << 22); ++spins) {
        if (__hip_atomic_load((__attribute__((address_space(3))) unsigned*)p, __ATOMIC_RELAXED, __HIP_MEMORY_SCOPE_WORKGROUP) != 0u) break;
        __builtin_amdgcn_s_sleep(1);
    }
    asm volatile("" ::: "memory");
}
#define WAIT_VM(n) asm volatile("s_waitcnt vmcnt(" #n ")" ::: "memory")
#define WAIT_VM_BAR(n) asm volatile("s_waitcnt vmcnt(" #n ") lgkmcnt(0)\n\ts_barrier" ::: "memory")
#define SBAR() __builtin_amdgcn_sched_barrier(0)
#define PIN(x) asm volatile("" : "+v"(x))
__device__ __forceinline__ u32x4 lds128(lptr p) { return *(LAS const u32x4*)p; }
__device__ __forceinline__ u32x2 lds64(lptr p) { return *(LAS const u32x2*)p; }
typedef short v4i16_t __attribute__((ext_vector_type(4)));
__device__ __forceinline__ u32x2 ldstr(lptr p) { return __builtin_bit_cast(u32x2, __builtin_amdgcn_ds_read_tr16_b64_v4i16((LAS v4i16_t*)p)); }
__device__ __forceinline__ float xhalf(float v) {
    return __shfl_xor(v, 32);
}
__device__ __forceinline__ int crow(int r, int hh) { return (r & 3) + 8 * (r >> 2) + 4 * hh; }
__device__ __forceinline__ size_t kvtile(int h, int tt) { return (size_t)h * KVHEAD + (tt >> 1) * KVPAIR + (tt & 1) * KVT; }

__device__ __forceinline__ int phi32(int rho) { const int r = (rho & 3) + 4 * (rho >> 3), hc = (rho >> 2) & 1; return 16 * (r >> 3) + 8 * hc + (r & 7); }
struct SideW { const u16 *wo, *wg, *wu, *wd; char* ws; };
struct SideJob {
    int type;
    const char* src[2];
    int pstep[4];
    unsigned voff;
    char* dst[2]; int dstep[4];
    int raddr[4];
    float* cs;
};
__device__ __forceinline__ SideJob side_job(const SideW& w, int item, int lane) {
    SideJob g;
    const int q4 = (lane >> 2) & 3, p4 = lane & 3, grp = lane >> 4;
    g.cs = nullptr; g.type = 3; g.voff = 0;
    int rs = D_ * 2;
    if (item < 1296) {
        const int U = item / 18, mat = (item % 18) / 9, sp = item % 9;
        g.type = 0; rs = F_ * 2;
        g.src[0] = (const char*)(mat ? w.wu : w.wg) + 128 * U + (size_t)(64 * sp) * rs; g.src[1] = g.src[0] + 32 * rs;
        g.dst[0] = w.ws + OFF_WGU + (size_t)((4 * U * 18 + 2 * sp) * 2 + mat) * 1024; g.dst[1] = g.dst[0] + 2048;
        g.cs = (float*)(w.ws + OFF_CSP) + (size_t)(sp * 288 + 4 * U) * 32 + mat * 16;
#pragma unroll
        for (int t = 0; t < 4; ++t) { g.pstep[t] = 8 * t * rs; g.dstep[t] = t * 36 * 1024; g.raddr[t] = (8 * grp + q4) * 128 + 32 * t + 8 * p4; }
        g.voff = (lane >> 3) * rs + (lane & 7) * 16;
    } else if (item < 1944) {
        const int it = item - 1296, V = it / 72, sp = it % 72;
        g.type = 1;
        g.src[0] = (const char*)w.wd + 128 * V + (size_t)(64 * sp) * rs; g.src[1] = g.src[0] + 32 * rs;
        g.dst[0] = w.ws + OFF_WDT + (size_t)(((V / 3) * 144 + 2 * sp) * 12 + (V % 3) * 4) * 1024; g.dst[1] = g.dst[0] + 12 * 1024;
#pragma unroll
        for (int t = 0; t < 4; ++t) { g.pstep[t] = 8 * t * rs; g.dstep[t] = t * 1024; g.raddr[t] = (8 * grp + q4) * 128 + 64 * (t >> 1) + 16 * p4 + 8 * (t & 1); }
        g.voff = (lane >> 3) * rs + (lane & 7) * 16;
    } else {
        const int it = item < 2025 ? item - 1944 : 0, V = it / 9, kq = it % 9;
        const bool mainp = kq < 8;
        const int jj = lane >> 3;
        if (item < 2025) g.type = 2;
#pragma unroll
        for (int b = 0; b < 2; ++b) {
            const int ks = 4 * kq + 2 * b;
            g.src[b] = (const char*)w.wo + 128 * V + (size_t)(mainp ? 18 * ks : 144 * (ks - 32)) * rs;
            g.dst[b] = w.ws + OFF_WOT + (size_t)(2 * V * 36 + ks) * 1024;
        }
#pragma unroll
        for (int t = 0; t < 4; ++t) {
            g.pstep[t] = (mainp ? 20 * (t >> 1) + 4 * (t & 1) : 144 * (t >> 1) + 72 * (t & 1)) * rs;
            g.dstep[t] = (t >> 1) * 1024 + (t & 1) * 36 * 1024;
            g.raddr[t] = (16 * (t >> 1) + 8 * (grp >> 1) + q4) * 128 + 64 * (t & 1) + 2 * phi32(16 * (grp & 1) + 4 * p4);
        }
        g.voff = (mainp ? 8 * (jj >> 2) + (jj & 3) : 18 * (jj >> 1) + (((jj >> 1) & 1) ? (jj & 1) : 16 + (jj & 1))) * rs + (lane & 7) * 16;
    }
    return g;
}
__device__ __forceinline__ void side_fetch(const SideJob& g, int b, unsigned ldsbuf) {
    if (g.type != 3) {
#pragma unroll
        for (int p = 0; p < 4; ++p) dma16(g.voff, g.src[b] + g.pstep[p], ldsbuf + p * 1024);
    }
}
__device__ __forceinline__ void side_finish(const SideJob& g, int b, lptr buf, f32x4 (&cs)[4], int lane) {
    if (g.type == 3) return;
    u32x4 v[4];
#pragma unroll
    for (int t = 0; t < 4; ++t) {
        const u32x2 lo = ldstr(buf + g.raddr[t]), hi = ldstr(buf + g.raddr[t] + 512);
        v[t] = (u32x4){lo[0], lo[1], hi[0], hi[1]};
    }
#pragma unroll
    for (int t = 0; t < 4; ++t) st16(g.dst[b] + g.dstep[t] + lane * 16, v[t]);
    if (g.type == 0) {
        const u32x4 ones = {0x3F803F80u, 0x3F803F80u, 0x3F803F80u, 0x3F803F80u};
#pragma unroll
        for (int t = 0; t < 4; ++t) cs[t] = mfma16(v[t], ones, cs[t]);
        if (b == 1 && (lane & 15) == 0) {
#pragma unroll
            for (int t = 0; t < 4; ++t) st16f(g.cs + t * 32 + (lane >> 4) * 4, cs[t]);
        }
    }
}

constexpr int K1_BUF = 32768, K1_NB = 4, K1_ST = K1_NB * K1_BUF;
constexpr float QSCALE = 0.23570226039551584f * 1.4426950408889634f;

constexpr int K1_RAW = K1_ST, K1_CS = K1_ST + 1024, K1_PV = K1_ST + 1536, K1_SIDE = K1_PV + 8 * 256, K1_LDS = K1_SIDE + 8192;
__device__ __forceinline__ void k1_finish(const f32x16& acc, int f, int t, bool isV, char* __restrict__ ws, lptr lds, int mt, int g, int wid, int lane) {
    const int hh = lane >> 5, l31 = lane & 31, tt = 4 * mt + t;
    char* qimg = ws + OFF_QIMG; char* kv = ws + OFF_KV;
    LAS const float* cs = (LAS const float*)(lds + K1_CS);
    const f32x2 sv = *(LAS const f32x2*)(lds + K1_RAW + (32 * t + l31) * 8);
    const float mean = sv[0] * (1.f / 576.f);
    const float rstd = __builtin_amdgcn_rsqf(fmaxf(sv[1] * (1.f / 576.f) - mean * mean, 0.f) + 1e-5f), mrs = mean * rstd;
    auto pair = [](const f32x16& a, int r) { return (f32x2){a[r], a[r + 1]}; };
    unsigned pk[8];
    if (isV) {
        LAS float* pv = (LAS float*)(lds + K1_PV + wid * 256);
        if (hh == 0) { pv[l31] = rstd; pv[32 + l31] = mrs; }
        const float c = -cs[32 * f + l31];
        asm volatile("s_waitcnt lgkmcnt(0)" ::: "memory");
#pragma unroll
        for (int q = 0; q < 4; ++q) {
            const f32x4 rs = *(LAS const f32x4*)(pv + 8 * q + 4 * hh), mr = *(LAS const f32x4*)(pv + 32 + 8 * q + 4 * hh);
            const f32x2 v0 = __builtin_elementwise_fma(pair(acc, 4 * q), (f32x2){rs[0], rs[1]}, (f32x2){mr[0], mr[1]} * (f32x2){c, c});
            const f32x2 v1 = __builtin_elementwise_fma(pair(acc, 4 * q + 2), (f32x2){rs[2], rs[3]}, (f32x2){mr[2], mr[3]} * (f32x2){c, c});
            pk[2 * q] = pk2(v0[0], v0[1]); pk[2 * q + 1] = pk2(v1[0], v1[1]);
        }
        asm volatile("s_waitcnt lgkmcnt(0)" ::: "memory");
        int h, d;
        if (f < 3) { h = 2 * ((3 * g + f) & 15) + (l31 >> 4); d = l31 & 15; }
        else if (l31 < 12) { h = 2 * ((3 * g + (l31 >> 2)) & 15) + ((l31 >> 1) & 1); d = 16 + (l31 & 1); }
        else {
            const int i = l31 < 18 ? l31 - 12 : 0;
            h = 2 * ((3 * g + (i >> 1)) & 15) + (i & 1); d = 18;
#pragma unroll
            for (int i2 = 0; i2 < 8; ++i2) pk[i2] = 0x3F803F80u;
        }
        if (f < 3 || l31 < 18) {
            char* dst = kv + kvtile(h, tt) + 1024 + hh * 304 + d * 16;
            st16(dst, (u32x4){pk[0], pk[1], pk[2], pk[3]});
            st16(dst + 608, (u32x4){pk[4], pk[5], pk[6], pk[7]});
        }
        return;
    }
    const int P0 = 3 * g + (f < 3 ? f : 0), sec0 = P0 >> 4;
    const float sc = (f < 3 && sec0 == 0) ? QSCALE : 1.f;
    const float rs = rstd * sc, nmr = -mrs * sc;
    f32x2 v[8];
#pragma unroll
    for (int q = 0; q < 4; ++q) {
        const f32x4 cq = *(LAS const f32x4*)(cs + 32 * f + 8 * q + 4 * hh);
        v[2 * q] = __builtin_elementwise_fma(pair(acc, 4 * q), (f32x2){rs, rs}, (f32x2){cq[0], cq[1]} * (f32x2){nmr, nmr});
        v[2 * q + 1] = __builtin_elementwise_fma(pair(acc, 4 * q + 2), (f32x2){rs, rs}, (f32x2){cq[2], cq[3]} * (f32x2){nmr, nmr});
    }
    if (f < 3) {
        const int pp = P0 & 15;
#pragma unroll
        for (int i = 0; i < 8; ++i) pk[i] = pk2(v[i][0], v[i][1]);
        char* base = sec0 == 0 ? qimg + (size_t)(2 * pp * 64 + tt) * QTILE : kv + kvtile(2 * pp, tt);
        const size_t hs = sec0 == 0 ? (size_t)64 * QTILE : (size_t)KVHEAD;
        st16(base + lane * 16, (u32x4){pk[0], pk[1], pk[2], pk[3]});
        st16(base + hs + lane * 16, (u32x4){pk[4], pk[5], pk[6], pk[7]});
    } else {
#pragma unroll
        for (int u = 0; u < 2; ++u) {
            const int p = 2 * u + hh;
            if (p < 3) {
                const int P = 3 * g + p, sec = P >> 4, pp = P & 15;
#pragma unroll
                for (int o = 0; o < 2; ++o) {
                    const int h = 2 * pp + o;
                    const f32x2 e = v[2 * u + o];
                    if (sec == 0) {
                        st4(qimg + (size_t)(h * 64 + tt) * QTILE + 1024 + l31 * 4, pk2(e[0] * QSCALE, e[1] * QSCALE));
                    } else if (sec == 1) {
                        st4(kv + (size_t)h * KVHEAD + (tt >> 1) * KVPAIR + KVTAIL + l31 * 8 + (tt & 1) * 4, pk2(e[0], e[1]));
                    } else {
                        const unsigned pq = pk2(e[0], e[1]);
                        const int off = (2 * (l31 >> 4) + ((l31 >> 2) & 1)) * 304 + (((l31 >> 3) & 1) * 4 + (l31 & 3)) * 2;
                        char* dst = kv + kvtile(h, tt) + 1024 + off;
                        *(u16*)(dst + 16 * 16) = (u16)(pq & 0xffff);
                        *(u16*)(dst + 17 * 16) = (u16)(pq >> 16);
                        *(u16*)(dst + 18 * 16) = (u16)0x3F80;
                    }
                }
            }
        }
    }
}

struct K1Frag { u32x4 x0, x1, w0, w1; };
template <bool V0, bool V1, bool T0, bool T1>
__device__ __forceinline__ void k1_body(char* __restrict__ ws, lptr lds, int mt, int g, int wid, int lane) {
    const int a = wid >> 1, b = wid & 1;
    const int hh = lane >> 5, l31 = lane & 31;
    const int t0 = 2 * b + a, t1 = 2 * b + (1 - a);
    const int f0 = 2 * a + b, f1 = 2 * a + (1 - b);
    const int sw = (l31 >> 1) & 7;
    const int xc = (hh ^ sw) << 4;
    int xa0[4], xa1[4];
#pragma unroll
    for (int s = 0; s < 4; ++s) { xa0[s] = (32 * t0 + l31) * 128 + (xc ^ (s << 5)); xa1[s] = (32 * t1 + l31) * 128 + (xc ^ (s << 5)); }
    const int q4 = (lane >> 2) & 3, p4 = lane & 3, cg = (lane >> 4) & 1;
    auto waddr = [&](int f) {
        const int c = f < 3 ? 4 * f + 2 * cg + (p4 >> 1) : 12 + p4;
        const int sub = f < 3 ? 8 * (p4 & 1) : 0;
        return 16384 + (8 * hh + q4) * 256 + ((c ^ (q4 << 2)) << 4) + sub;
    };
    const int wa0 = waddr(f0), wa1 = waddr(f1);
    const u32x4 ones = {0x3F803F80u, 0x3F803F80u, 0x3F803F80u, 0x3F803F80u};
    const bool onesrow = l31 >= 12;

    f32x16 acc00 = {}, acc01 = {}, acc10 = {}, acc11 = {}, accG = {};
    f32x4 accC = {};
    const unsigned pick = ((lane & 15) == ((lane >> 4) & 1)) ? 0x3F803F80u : 0u;
    const u32x4 sumA = {pick, pick, pick, pick};
    auto step = [&](K1Frag& C, K1Frag& N, lptr nb, int nks) {
        u32x2 lo, hi;
        if (T0) { for (int i = 0; i < 4; ++i) C.w0[i] = onesrow ? ones[i] : C.w0[i]; }
        if (T1) { for (int i = 0; i < 4; ++i) C.w1[i] = onesrow ? ones[i] : C.w1[i]; }
        acc00 = V0 ? mfma32(C.x0, C.w0, acc00) : mfma32(C.w0, C.x0, acc00); PIN(acc00); SBAR();
        N.x0 = lds128(nb + xa0[nks]); SBAR();
        accG = mfma32(C.x0, C.x0, accG); PIN(accG); SBAR();
        lo = ldstr(nb + wa0 + nks * 4096); SBAR();
        acc01 = V0 ? mfma32(C.x1, C.w0, acc01) : mfma32(C.w0, C.x1, acc01); PIN(acc01); SBAR();
        hi = ldstr(nb + wa0 + nks * 4096 + 1024); SBAR();
        acc10 = V1 ? mfma32(C.x0, C.w1, acc10) : mfma32(C.w1, C.x0, acc10); PIN(acc10); SBAR();
        N.x1 = lds128(nb + xa1[nks]); SBAR();
        accC = mfma16(sumA, C.w0, accC); PIN(accC); SBAR();
        N.w0 = (u32x4){lo[0], lo[1], hi[0], hi[1]};
        lo = ldstr(nb + wa1 + nks * 4096); SBAR();
        acc11 = V1 ? mfma32(C.x1, C.w1, acc11) : mfma32(C.w1, C.x1, acc11); PIN(acc11); SBAR();
        hi = ldstr(nb + wa1 + nks * 4096 + 1024);
        N.w1 = (u32x4){lo[0], lo[1], hi[0], hi[1]};
        SBAR();
    };
    K1Frag F0, F1, F2, F3;
    auto rdall = [&](K1Frag& N, int ks) {
        N.x0 = lds128(lds + xa0[ks]); N.x1 = lds128(lds + xa1[ks]);
        const u32x2 a0 = ldstr(lds + wa0 + ks * 4096), a1 = ldstr(lds + wa0 + ks * 4096 + 1024), b0 = ldstr(lds + wa1 + ks * 4096), b1 = ldstr(lds + wa1 + ks * 4096 + 1024);
        N.w0 = (u32x4){a0[0], a0[1], a1[0], a1[1]}; N.w1 = (u32x4){b0[0], b0[1], b1[0], b1[1]};
    };
    asm volatile("s_barrier" ::: "memory");
    rdall(F0, 0); rdall(F1, 1);
    for (int s = 0; s < 9; ++s) {
        lptr buf = lds + (s & 3) * K1_BUF, nbuf = lds + ((s + 1) & 3) * K1_BUF;
        step(F0, F2, buf, 2);
        step(F1, F3, buf, 3);
        asm volatile("s_barrier" ::: "memory");
        step(F2, F0, nbuf, 0);
        step(F3, F1, nbuf, 1);
    }
    TS(1, 3);
    {
        LAS float* st = (LAS float*)(lds + K1_RAW);
        LAS float* cs = (LAS float*)(lds + K1_CS);
        LAS f32x4* ho = (LAS f32x4*)(lds + wid * 8192) + lane;
#pragma unroll
        for (int q = 0; q < 4; ++q) {
            ho[q * 64] = (f32x4){acc10[4 * q], acc10[4 * q + 1], acc10[4 * q + 2], acc10[4 * q + 3]};
            ho[(4 + q) * 64] = (f32x4){acc11[4 * q], acc11[4 * q + 1], acc11[4 * q + 2], acc11[4 * q + 3]};
        }
        float dg = accG[0];
        const int rsel = (l31 & 3) + 4 * (l31 >> 3);
#pragma unroll
        for (int r = 1; r < 16; ++r) dg = rsel == r ? accG[r] : dg;
        if (hh == ((l31 >> 2) & 1)) st[(32 * t0 + l31) * 2 + 1] = dg;
        if (lane < 16) { cs[32 * f0 + lane] = accC[0]; cs[32 * f0 + 16 + lane] = accC[1]; }
        auto rowsum = [&](const f32x16& acc, int t, bool isV) {
            if (!isV) { if (hh == 1) st[(32 * t + l31) * 2] = acc[4]; }
            else if (l31 == 12) {
#pragma unroll
                for (int r = 0; r < 16; ++r) st[(32 * t + crow(r, hh)) * 2] = acc[r];
            }
        };
        if (T0) { rowsum(acc00, t0, V0); rowsum(acc01, t1, V0); }
        if (T1) { rowsum(acc10, t0, V1); rowsum(acc11, t1, V1); }
    }
    asm volatile("s_waitcnt lgkmcnt(0)\n\ts_barrier" ::: "memory");
    TS(1, 4);
    k1_finish(acc00, f0, t0, V0, ws, lds, mt, g, wid, lane);
    k1_finish(acc01, f0, t1, V0, ws, lds, mt, g, wid, lane);
    TS(1, 5); TR(1, 15);
}

__global__ __launch_bounds__(512, 2) void k1_qkv(const u16* __restrict__ x, const u16* __restrict__ w, const u16* __restrict__ wo, char* __restrict__ ws) {
    __shared__ __attribute__((aligned(16))) char smem[K1_LDS];
    const int lane = threadIdx.x & 63, wid = __builtin_amdgcn_readfirstlane(threadIdx.x >> 6);
    const int bx = blockIdx.x, xcd = bx & 7, j = bx >> 3;
    const int mt = (xcd >> 1) * 4 + (j & 3), g = (xcd & 1) * 8 + (j >> 2);
    lptr lds = (lptr)smem;
    TS(1, 0); TR(1, 14);
    if (bx < NFLAG / 512) ((unsigned*)(ws + OFF_FLAG))[bx * 512 + threadIdx.x] = 0u;
    if (wid >= 4) {
        const int l = wid - 4;
        const unsigned ldsbase = (unsigned)(uintptr_t)lds;
        unsigned xoff[4], woff[4];
#pragma unroll
        for (int i = 0; i < 4; ++i) {
            const int p = 4 * l + i;
            const int row = 8 * p + (lane >> 3), ch = (lane & 7) ^ ((row >> 1) & 7);
            xoff[i] = row * 1152 + ch * 16;
            const int kr = 4 * p + (lane >> 4), c = (lane & 15) ^ ((kr & 3) << 2);
            const int cs = c & 3;
            const int byte = c < 12 ? 72 * (c >> 2) + (cs < 2 ? 16 * cs : 40 + 16 * (cs - 2)) : 72 * ((c < 14 ? c : 14) - 12) + 32;
            woff[i] = kr * 3456 + byte;
        }
        const char* xb = (const char*)x + (size_t)mt * 128 * 1152;
        const char* wb = (const char*)w + g * 216;
        auto issue = [&](int s) {
            const unsigned dst = ldsbase + (s & 3) * K1_BUF + l * 4096;
#pragma unroll
            for (int i = 0; i < 4; ++i) {
                dma16(xoff[i], xb + s * 128, dst + i * 1024);
                dma16(woff[i], wb + (size_t)s * 64 * 3456, dst + 16384 + i * 1024);
            }
        };
        issue(0); issue(1);
        const bool side = l == 3 && bx < 81;
        const SideJob job = side_job(SideW{wo, wo, wo, wo, ws}, side ? 1944 + bx : 2047, lane);
        side_fetch(job, 0, ldsbase + K1_SIDE); side_fetch(job, 1, ldsbase + K1_SIDE + 4096);
        TS(1, 1);
        if (side) WAIT_VM(16); else WAIT_VM(8);
        asm volatile("s_barrier ; #0" ::: "memory");
        TS(1, 2);
        issue(2);
        for (int s = 1; s < 9; ++s) {
            if (s < 8) WAIT_VM(8); else WAIT_VM(0);
            __builtin_amdgcn_s_barrier();
            if (s < 7) issue(s + 2);
            if (s == 2) {
                f32x4 cs[4] = {};
                side_finish(job, 0, lds + K1_SIDE, cs, lane); side_finish(job, 1, lds + K1_SIDE + 4096, cs, lane);
            }
        }
        __builtin_amdgcn_s_barrier();
        asm volatile("s_barrier" ::: "memory");
        {
            const int a = l >> 1, b = l & 1, t0 = 2 * b + a, t1 = 2 * b + (1 - a), f1 = 2 * a + (1 - b);
            const bool isV = f1 < 3 ? 3 * g + f1 >= 32 : 3 * g >= 32;
            LAS const f32x4* ho = (LAS const f32x4*)(lds + l * 8192) + lane;
#pragma unroll
            for (int i = 0; i < 2; ++i) {
                f32x16 acc;
#pragma unroll
                for (int q = 0; q < 4; ++q) { const f32x4 v = ho[(4 * i + q) * 64]; acc[4 * q] = v[0]; acc[4 * q + 1] = v[1]; acc[4 * q + 2] = v[2]; acc[4 * q + 3] = v[3]; }
                k1_finish(acc, f1, i == 0 ? t0 : t1, isV, ws, lds, mt, g, wid, lane);
            }
        }
        TS(1, 5); TR(1, 15);
        return;
    }
    const int a = wid >> 1, b = wid & 1;
    const int f0 = 2 * a + b;
    const bool vA = 3 * g >= 32, vB = 3 * g + 2 >= 32;
    if (a == 0) { if (vA) k1_body<true, true, false, false>(ws, lds, mt, g, wid, lane); else k1_body<false, false, false, false>(ws, lds, mt, g, wid, lane); }
    else if (f0 == 2) {
        if (vA) k1_body<true, true, false, true>(ws, lds, mt, g, wid, lane);
        else if (vB) k1_body<true, false, false, true>(ws, lds, mt, g, wid, lane);
        else k1_body<false, false, false, true>(ws, lds, mt, g, wid, lane);
    } else {
        if (vA) k1_body<true, true, true, false>(ws, lds, mt, g, wid, lane);
        else if (vB) k1_body<false, true, true, false>(ws, lds, mt, g, wid, lane);
        else k1_body<false, false, true, false>(ws, lds, mt, g, wid, lane);
    }
}

struct K2Regs { u32x4 km, v0, v1; u32x2 kt; float e0, e1; };
struct NoHook { template <class T> __device__ __forceinline__ void operator()(T) const {} };
template <int N> using IC = std::integral_constant<int, N>;
template <bool ODD, bool QKN, bool LDKN, bool LDVN, class H>
__device__ __forceinline__ void k2_step(f32x16& SC, f32x16& SN, f32x16& o, K2Regs& R, const u32x4& qm, const u32x2& qtA, const u32x2& qtB,
                                        lptr kp, lptr ktp, lptr vp, H&& hook) {
    float e[16]; unsigned pw[8];
#define EX(i) do { e[i] = __builtin_amdgcn_exp2f(SC[i]); PIN(e[i]); } while (0)
#define CV(i) do { pw[i] = pk2(e[2 * (i)], e[2 * (i) + 1]); PIN(pw[i]); } while (0)
    e[0] = R.e0; e[1] = R.e1;
    EX(2); EX(3); EX(4); EX(5);
    SBAR();
    if constexpr (QKN) { SN = mfma32k8(R.kt, ODD ? qtA : qtB, (f32x16){}); PIN(SN); SBAR(); }
    hook(IC<0>{});
    EX(6); EX(7); CV(0); CV(1); CV(2); CV(3);
    SBAR();
    if constexpr (QKN) { SN = mfma32(R.km, qm, SN); PIN(SN); SBAR(); }
    if constexpr (LDKN) {
        R.km = lds128(kp + KVPAIR + (ODD ? KVT : 0));
        if constexpr (!ODD) R.kt = lds64(ktp + KVPAIR);
    }
    EX(8); EX(9); EX(10); EX(11);
    SBAR();
    asm volatile("" :: "v"(R.v0), "v"(R.v1));
    o = mfma32(R.v0, (u32x4){pw[0], pw[1], pw[2], pw[3]}, o); PIN(o); SBAR();
    hook(IC<1>{});
    EX(12); EX(13); EX(14); EX(15); CV(4); CV(5); CV(6); CV(7);
    SBAR();
    if constexpr (QKN) { R.e0 = __builtin_amdgcn_exp2f(SN[0]); PIN(R.e0); R.e1 = __builtin_amdgcn_exp2f(SN[1]); PIN(R.e1); }
    else { asm volatile("s_nop 1"); }
    SBAR();
    o = mfma32(R.v1, (u32x4){pw[4], pw[5], pw[6], pw[7]}, o); PIN(o); SBAR();
    if constexpr (LDVN) { R.v0 = lds128(vp + (ODD ? KVPAIR : KVT)); R.v1 = lds128(vp + (ODD ? KVPAIR : KVT) + 608); }
    SBAR();
#undef EX
#undef CV
}

template <int I>
__device__ __forceinline__ void k2_piece(int c, int wid, unsigned voff, const char* kvsrc, unsigned ldsbase) {
    int off = 40960 + 32768 * c + 4096 * wid;
    off = off + 4096 <= KVHEAD ? off : KVHEAD - 4096;
    if constexpr (I == 0) asm volatile("s_mov_b32 m0, %2\n\ts_nop 0\n\tglobal_load_lds_dwordx4 %0, %1" :: "v"(voff), "s"(kvsrc + off), "s"(ldsbase + off) : "memory");
    else asm volatile("global_load_lds_dwordx4 %0, %1 offset:%2" :: "v"(voff), "s"(kvsrc + off), "i"(I * 1024) : "memory");
}
template <int K>
__device__ __forceinline__ void k3_steps(f32x16& acc, u32x4 (&af)[36], u32x4 (&b)[4], lptr bp) {
    if constexpr (K < 36) {
        acc = mfma32(af[K], b[K & 3], K == 0 ? (f32x16){} : acc); PIN(acc);
        if constexpr (K + 4 < 36) b[K & 3] = lds128(bp + (K + 4) * 1024);
        SBAR();
        k3_steps<K + 1>(acc, af, b, bp);
    }
}
template <int K>
__device__ __forceinline__ void k3_pin(u32x4 (&af)[36]) { if constexpr (K < 36) { PIN(af[K]); k3_pin<K + 1>(af); } }
template <int A>
__device__ __forceinline__ void k3_aload(u32x4 (&af)[36], const char* wsrc, unsigned voff) {
    asm volatile("global_load_dwordx4 %0, %1, %2 offset:%3" : "=v"(af[A]) : "v"(voff), "s"(wsrc + (A / 4) * 4096), "i"((A & 3) * 1024) : "memory");
}

constexpr int K4_SLOT = 36 * 1024, K4_CS = 4 * K4_SLOT, K4_NB = 4;
#define K4_XD 12

struct K4Acc { f32x4 g[2], u[2]; };
struct K4Epi { float g[8], u[8], e[8]; unsigned pk[4]; f32x4 cg, cu; };
constexpr int K4_NCH = 8, K4_NOP = 11;
struct K4Ctx {
    lptr slot[4]; LAS const float* csw; float m[2], C0[2], C1[2], V[2];
    const char* wsrc; unsigned ldsbase; int wid; unsigned voff, voffx, voffa; const char* hsrc; char* adst; bool young;
};
template <int R, int OP>
__device__ __forceinline__ void k4_epi_op(K4Epi& E, const K4Acc& acc, const K4Ctx& c) {
    constexpr int U = R >> 2, Q = R & 3;
    if constexpr (OP == 0) { E.g[R] = __builtin_fmaf(-c.m[U], E.cg[Q], acc.g[U][Q]); PIN(E.g[R]); }
    else if constexpr (OP == 1) { E.u[R] = __builtin_fmaf(-c.m[U], E.cu[Q], acc.u[U][Q]); PIN(E.u[R]); }
    else if constexpr (OP == 2) { E.e[R] = E.g[R] * E.g[R]; PIN(E.e[R]); }
    else if constexpr (OP == 3) { E.e[R] = __builtin_fmaf(E.e[R], c.C1[U], c.C0[U]); PIN(E.e[R]); }
    else if constexpr (OP == 4) { E.e[R] = E.g[R] * E.e[R]; PIN(E.e[R]); }
    else if constexpr (OP == 5) { E.e[R] = __builtin_amdgcn_exp2f(E.e[R]); PIN(E.e[R]); }
    else if constexpr (OP == 6) { E.e[R] = __builtin_fmaf(E.e[R], c.V[U], c.V[U]); PIN(E.e[R]); }
    else if constexpr (OP == 7) { E.e[R] = __builtin_amdgcn_rcpf(E.e[R]); PIN(E.e[R]); }
    else if constexpr (OP == 8) { E.u[R] = E.g[R] * E.u[R]; PIN(E.u[R]); }
    else if constexpr (OP == 9) { E.u[R] = E.u[R] * E.e[R]; PIN(E.u[R]); }
    else if constexpr (R & 1) { E.pk[R >> 1] = pk2(E.u[R - 1], E.u[R]); PIN(E.pk[R >> 1]); }
}
template <int M, int START, int CSP, int OSP, int R>
__device__ __forceinline__ void k4_epi_slot(K4Epi& E, const K4Acc& acc, const K4Ctx& c) {
    if constexpr (R < K4_NCH) {
        constexpr int d = M - (START + CSP * R);
        if constexpr (d >= 0 && d <= (K4_NOP - 1) * OSP && d % OSP == 0) k4_epi_op<R, d / OSP>(E, acc, c);
        k4_epi_slot<M, START, CSP, OSP, R + 1>(E, acc, c);
    }
}
__device__ __forceinline__ void k4_epi_begin(K4Epi& E, const K4Ctx& c, int f) {
    LAS const float* cs = c.csw + f * 32;
    E.cg = *(LAS const f32x4*)(cs); E.cu = *(LAS const f32x4*)(cs + 16);
}
template <int U>
__device__ __forceinline__ void k4_store(const K4Epi& E, const K4Ctx& c, int f) {
    st8(c.adst + (size_t)f * 1024 + U * 256 + c.voffa, (u32x2){E.pk[2 * U], E.pk[2 * U + 1]});
}
template <int I>
__device__ __forceinline__ void k4_dma(const K4Ctx& c, int f) {
    const char* src = c.wsrc + (size_t)f * K4_SLOT + c.wid * 4096;
    if constexpr (I == 0)
        asm volatile("s_mov_b32 m0, %2\n\ts_nop 0\n\tglobal_load_lds_dwordx4 %0, %1 sc1" :: "v"(c.voff), "s"(src), "s"(c.ldsbase + (f & 3) * K4_SLOT + c.wid * 4096) : "memory");
    else if constexpr (I < 4)
        asm volatile("global_load_lds_dwordx4 %0, %1 offset:%2 sc1" :: "v"(c.voff), "s"(src), "i"(I * 1024) : "memory");
    else if (c.wid < 4)
        dma1k_dev(c.voff, c.wsrc + (size_t)f * K4_SLOT + (32 + c.wid) * 1024, c.ldsbase + (f & 3) * K4_SLOT + (32 + c.wid) * 1024);
}
#define K4_SW 0
#define K4_SWA 0
template <int S, int SW>
__device__ __forceinline__ void k4_prio(const K4Ctx& c) {
    if constexpr (SW > 0 && S == 0) { if (c.young) __builtin_amdgcn_s_setprio(1); }
    if constexpr (SW > 0 && S == SW) { if (c.young) __builtin_amdgcn_s_setprio(0); }
}
template <int N>
__device__ __forceinline__ void k4_xload(u32x4 (&xf)[36], const K4Ctx& c) {
    asm volatile("global_load_dwordx4 %0, %1, %2 offset:%3 sc1" : "=v"(xf[N]) : "v"(c.voffx), "s"(c.hsrc + (N / 4) * 4096), "i"(((N >> 1) & 1) * 2048 + (N & 1) * 256) : "memory");
}
constexpr int k4_xissued(int s) {
    if (s < 0) return 0;
    int a = k4_xissued(s - 1) + 6, b = 2 * (s + 1) + K4_XD;
    a = a < b ? a : b;
    return a < 36 ? a : 36;
}
template <int A, int B>
__device__ __forceinline__ void k4_xloads(u32x4 (&xf)[36], const K4Ctx& c) {
    if constexpr (A < B) { k4_xload<A>(xf, c); k4_xloads<A + 1, B>(xf, c); }
}
template <int S, class ST>
__device__ __forceinline__ void k4_first(K4Acc& acc0, K4Acc& acc1, K4Acc& acc2, u32x4 (&a)[6], u32x4 (&xf)[36], const K4Ctx& c, ST&& stats) {
    if constexpr (S < 18) {
        k4_prio<S, K4_SWA>(c);
        constexpr int i0 = k4_xissued(S - 1), i1 = k4_xissued(S);
        k4_xloads<i0, i1>(xf, c);
        if constexpr (S == 0) stats();
        asm volatile("s_waitcnt vmcnt(%2)" : "+v"(xf[2 * S]), "+v"(xf[2 * S + 1]) : "i"(i1 - (2 * S + 2)) : "memory");
        auto pair = [&](f32x4& d0, f32x4& d1, auto idx) {
            constexpr int i = decltype(idx)::value;
            d0 = mfma16(a[i], xf[2 * S], S == 0 ? (f32x4){} : d0); PIN(d0); SBAR();
            d1 = mfma16(a[i], xf[2 * S + 1], S == 0 ? (f32x4){} : d1); PIN(d1);
            if constexpr (S < 17) a[i] = lds128(c.slot[i >> 1] + (2 * (S + 1) + (i & 1)) * 1024);
            SBAR();
        };
        pair(acc0.g[0], acc0.g[1], std::integral_constant<int, 0>{});
        pair(acc0.u[0], acc0.u[1], std::integral_constant<int, 1>{});
        if constexpr (S % 2 == 1 && S / 2 < 5) k4_dma<S / 2>(c, 3);
        pair(acc1.g[0], acc1.g[1], std::integral_constant<int, 2>{});
        pair(acc1.u[0], acc1.u[1], std::integral_constant<int, 3>{});
        pair(acc2.g[0], acc2.g[1], std::integral_constant<int, 4>{});
        pair(acc2.u[0], acc2.u[1], std::integral_constant<int, 5>{});
        k4_first<S + 1>(acc0, acc1, acc2, a, xf, c, stats);
    }
}
template <int F, int S>
__device__ __forceinline__ void k4_steps(K4Acc& acc, const K4Acc& accP, const K4Acc& accQ, u32x4 (&a)[K4_NB], u32x4 (&xf)[36], K4Epi& E, const K4Ctx& c) {
    if constexpr (S < 18) {
        constexpr int ig = (2 * S) % K4_NB, iu = (2 * S + 1) % K4_NB;
        auto refill = [&](auto idx, auto frag) {
            constexpr int i = decltype(idx)::value, n = decltype(frag)::value;
            if constexpr (n < 36) a[i] = lds128(c.slot[F & 3] + n * 1024);
            else if constexpr (F >= 4 && F < 8) a[i] = lds128(c.slot[(F + 1) & 3] + (n - 36) * 1024);
        };
        auto epi = [&](auto slot) {
            constexpr int M = decltype(slot)::value;
            if constexpr (F == 3) {
                if constexpr (M == 0) k4_epi_begin(E, c, 1);
                if constexpr (M <= 43) k4_epi_slot<M, 2, 3, 2, 0>(E, accP, c);
                if constexpr (M == 26) k4_epi_begin(E, c, 2);
                if constexpr (M >= 30) k4_epi_slot<M, 30, 3, 2, 0>(E, accQ, c);
                if constexpr (M == 33) k4_store<0>(E, c, 1);
                if constexpr (M == 44) k4_store<1>(E, c, 1);
                if constexpr (M == 61) k4_store<0>(E, c, 2);
                if constexpr (M == 71) k4_store<1>(E, c, 2);
            } else {
                if constexpr (M == 0) k4_epi_begin(E, c, F - 1);
                k4_epi_slot<M, 6, 6, 2, 0>(E, accP, c);
                if constexpr (M == 51) k4_store<0>(E, c, F - 1);
                if constexpr (M == 71) k4_store<1>(E, c, F - 1);
            }
        };
        k4_prio<S, K4_SW>(c);
        asm volatile("" :: "v"(a[ig]), "v"(a[iu]));
        acc.g[0] = mfma16(a[ig], xf[2 * S], S == 0 ? (f32x4){} : acc.g[0]); PIN(acc.g[0]);
        epi(std::integral_constant<int, 4 * S>{});
        SBAR();
        acc.g[1] = mfma16(a[ig], xf[2 * S + 1], S == 0 ? (f32x4){} : acc.g[1]); PIN(acc.g[1]);
        refill(std::integral_constant<int, ig>{}, std::integral_constant<int, 2 * S + K4_NB>{});
        epi(std::integral_constant<int, 4 * S + 1>{});
        SBAR();
        acc.u[0] = mfma16(a[iu], xf[2 * S], S == 0 ? (f32x4){} : acc.u[0]); PIN(acc.u[0]);
        if constexpr (F == 3 || F == 4) { if constexpr (S < 10) k4_dma<S % 5>(c, 2 * F - 2 + S / 5); }
        else if constexpr (F == 5) { if constexpr (S % 2 == 0 && S / 2 < 5) k4_dma<S / 2>(c, 8); }
        epi(std::integral_constant<int, 4 * S + 2>{});
        SBAR();
        acc.u[1] = mfma16(a[iu], xf[2 * S + 1], S == 0 ? (f32x4){} : acc.u[1]); PIN(acc.u[1]);
        refill(std::integral_constant<int, iu>{}, std::integral_constant<int, 2 * S + 1 + K4_NB>{});
        epi(std::integral_constant<int, 4 * S + 3>{});
        SBAR();
        k4_steps<F, S + 1>(acc, accP, accQ, a, xf, E, c);
    }
}
template <int M>
__device__ __forceinline__ void k4_epi_all(K4Epi& E, const K4Acc& acc, const K4Ctx& c) {
    if constexpr (M < 40) { k4_epi_slot<M, 0, 3, 1, 0>(E, acc, c); k4_epi_all<M + 1>(E, acc, c); }
}

constexpr int KB_OT = 3 * K4_SLOT, KB_RED = K4_CS + 1152, KB_CNT = KVHEAD, KB_STAT = KVHEAD + 64, KB_LDS = KB_STAT + 8192;
static_assert(KB_RED + 5 * 32 * 8 <= KB_CNT && KB_LDS <= 163840, "");

__global__ __launch_bounds__(512, 2) void kb_fused(const u16* __restrict__ x, const u16* __restrict__ wg, const u16* __restrict__ wu,
                                                   const u16* __restrict__ wd, char* __restrict__ ws) {
    __shared__ __attribute__((aligned(16))) char smem[KB_LDS];
    lptr lds = (lptr)smem;
    const int bx = blockIdx.x, xcd = bx & 7, j = bx >> 3;
    const int member = 4 * xcd + (j >> 3), qb = j & 7;
    const int wid = __builtin_amdgcn_readfirstlane(threadIdx.x >> 6);
    const unsigned ldsbase = (unsigned)(uintptr_t)lds;
    const int cgB = member & 3, nT = cgB < 2 ? 5 : 4, dTB = (cgB < 2 ? 5 * cgB : 10 + 4 * (cgB - 2)) + (wid < nT ? wid : 0);
    u32x4 af[36];
    {
    const int lane = threadIdx.x & 63;
    const int h = member, tt = 8 * qb + wid;
    const int hh = lane >> 5, l31 = lane & 31;
    TS(2, 0); TR(2, 14);
    const unsigned voff = lane * 16;
    const char* kvsrc = ws + OFF_KV + (size_t)h * KVHEAD;
    {
        const char* s0 = kvsrc + wid * 5120; const unsigned d0 = ldsbase + wid * 5120;
        asm volatile("s_mov_b32 m0, %2\n\ts_nop 0\n\tglobal_load_lds_dwordx4 %0, %1" :: "v"(voff), "s"(s0), "s"(d0) : "memory");
    }
    u32x4 qm; unsigned qt;
    {
        const char* qsrc = ws + OFF_QIMG + (size_t)(h * 64 + tt) * QTILE;
        const unsigned o2 = 1024 + l31 * 4;
        asm volatile("global_load_dwordx4 %0, %2, %4\n\tglobal_load_dword %1, %3, %4" : "=&v"(qm), "=&v"(qt) : "v"(voff), "v"(o2), "s"(qsrc) : "memory");
    }
    {
        const char* s0 = kvsrc + wid * 5120; const unsigned d0 = ldsbase + wid * 5120;
        asm volatile("global_load_lds_dwordx4 %0, %1 offset:1024\n\tglobal_load_lds_dwordx4 %0, %1 offset:2048\n\tglobal_load_lds_dwordx4 %0, %1 offset:3072"
                     :: "v"(voff), "s"(s0) : "memory");
        dma16(voff, s0 + 4096, d0 + 4096);
    }
    lptr kp = lds + lane * 16, ktp = lds + KVTAIL + l31 * 8, vp = lds + 1024 + hh * 304 + l31 * 16;
    const bool young = wid >= 4;
#define PRIO(p) do { if (young) __builtin_amdgcn_s_setprio(p); } while (0)
    if (threadIdx.x < 4) *(LAS unsigned*)(lds + KB_CNT + 4 * threadIdx.x) = 0u;
    const SideW sw{wg, wg, wu, wd, ws};
    const int gw = bx * 8 + wid;
    const SideJob job = side_job(sw, gw < 1944 ? gw : 2047, lane);
    asm volatile("s_waitcnt vmcnt(0) lgkmcnt(0)\n\ts_barrier" : "+v"(qm), "+v"(qt) :: "memory");
    TS(2, 1);
    PRIO(1);
    const u32x2 qtA = {hh == 0 ? qt : 0u, 0u}, qtB = {0u, hh == 0 ? qt : 0u};
    f32x16 o = {}, sA, sB;
    K2Regs R;
    R.km = lds128(kp); R.kt = lds64(ktp);
    sA = mfma32(R.km, qm, mfma32k8(R.kt, qtA, (f32x16){}));
    R.km = lds128(kp + KVT);
    R.v0 = lds128(vp); R.v1 = lds128(vp + 608);
    R.e0 = __builtin_amdgcn_exp2f(sA[0]); R.e1 = __builtin_amdgcn_exp2f(sA[1]);
#define TWO(HA, HB) do { \
        k2_step<false, true, true, true>(sA, sB, o, R, qm, qtA, qtB, kp, ktp, vp, HA); \
        k2_step<true, true, true, true>(sB, sA, o, R, qm, qtA, qtB, kp, ktp, vp, HB); \
        kp += KVPAIR; ktp += KVPAIR; vp += KVPAIR; } while (0)
    const NoHook nh;
#define TWO_DMA(c) TWO(([&](auto p) { k2_piece<decltype(p)::value>(c, wid, voff, kvsrc, ldsbase); }), ([&](auto p) { k2_piece<2 + decltype(p)::value>(c, wid, voff, kvsrc, ldsbase); }))
    TWO_DMA(0); TWO_DMA(1); TWO_DMA(2); TWO_DMA(3);
#define K2_SW0 10
    if (K2_SW0 == 8) PRIO(0);
    TWO(nh, nh);
    if (K2_SW0 == 10) PRIO(0);
    TWO(nh, nh);
    if (K2_SW0 == 12) PRIO(0);
    TWO(nh, nh);
    asm volatile("s_waitcnt vmcnt(0)\n\ts_barrier" ::: "memory");
    PRIO(1);
    TS(2, 2);
    const lptr sbuf = lds + wid * 4096;
    f32x4 cs[4] = {};
    side_fetch(job, 0, ldsbase + wid * 4096);
    int t = 14;
#define K2_S1 24
#define K2_S2 34
    for (; t < K2_S1; t += 2) TWO(nh, nh);
    WAIT_VM(0);
    side_finish(job, 0, sbuf, cs, lane);
    asm volatile("s_waitcnt lgkmcnt(0)" ::: "memory");
    side_fetch(job, 1, ldsbase + wid * 4096);
    for (; t < K2_S2; t += 2) TWO(nh, nh);
    asm volatile("s_waitcnt vmcnt(0)" ::: "memory");
    side_finish(job, 1, sbuf, cs, lane);
    TS(2, 3);
#define K2_SW 52
    for (; t < K2_S2 + 6; t += 2) TWO(nh, nh);
    WAIT_VM(0);
    if (lane == 0) {
        if (__hip_atomic_fetch_add((LAS unsigned*)(lds + KB_CNT), 1u, __ATOMIC_RELAXED, __HIP_MEMORY_SCOPE_WORKGROUP) == 7u) flag_add(ws, FL_PRE + (bx & 15) * 32);
    }
    static_assert(K2_S2 + 6 == 40 && K2_SW == 52, "");
    if (wid < nT) {
        const char* wsrc = ws + OFF_WOT + (size_t)dTB * 36 * 1024;
#define AF2(n) TWO(([&](auto p) { k3_aload<(n) + decltype(p)::value>(af, wsrc, voff); }), ([&](auto p) { k3_aload<(n) + 2 + decltype(p)::value>(af, wsrc, voff); }))
        AF2(0); AF2(4); AF2(8); AF2(12); AF2(16); AF2(20);
        PRIO(0);
        AF2(24); AF2(28); AF2(32);
#undef AF2
        t = 58;
        asm volatile("; with prefetch");
    } else {
#pragma unroll 2
        for (; t < K2_SW; t += 2) TWO(nh, nh);
        PRIO(0);
        for (; t < 58; t += 2) TWO(nh, nh);
        asm volatile("; without prefetch");
    }
    for (; t < 62; t += 2) TWO(nh, nh);
    k2_step<false, true, false, true>(sA, sB, o, R, qm, qtA, qtB, kp, ktp, vp, nh);
    k2_step<true, false, false, false>(sB, sA, o, R, qm, qtA, qtB, kp, ktp, vp, nh);
#undef TWO
#undef TWO_DMA
#undef PRIO
    TS(2, 4);
    float l = __shfl(o[10], l31);
    float fin = 0.f;
#pragma unroll
    for (int r = 0; r < 8; ++r) fin = __builtin_fmaf(o[r], 0.f, fin);
    if (hh == 0) fin = __builtin_fmaf(o[8], 0.f, __builtin_fmaf(o[9], 0.f, fin));
    const bool bad = !(l > 8.673617379884035e-19f && l < 1.152921504606847e18f) || fin != 0.f;
    if (__builtin_expect(__any(bad), 0)) {
        o = (f32x16){}; float m = -3.0e38f;
        const char* kq = kvsrc + lane * 16; const char* ktq = kvsrc + KVTAIL + l31 * 8; const char* vq = kvsrc + 1024 + hh * 304 + (l31 < 18 ? l31 : 18) * 16;
        for (int tb = 0; tb < 64; ++tb) {
            const int off = (tb >> 1) * KVPAIR, sub = (tb & 1) * KVT;
            const u32x4 km = *(const u32x4*)(kq + off + sub), v0 = *(const u32x4*)(vq + off + sub), v1 = *(const u32x4*)(vq + off + sub + 608);
            const u32x2 kt = *(const u32x2*)(ktq + off);
            const f32x16 s = mfma32(km, qm, mfma32k8(kt, (tb & 1) ? qtB : qtA, (f32x16){}));
            float mx = s[0];
#pragma unroll
            for (int r = 1; r < 16; ++r) mx = fmaxf(mx, s[r]);
            mx = fmaxf(mx, xhalf(mx));
            const float mn = fmaxf(m, mx), al = __builtin_amdgcn_exp2f(m - mn);
            m = mn;
            unsigned pw[8];
#pragma unroll
            for (int i = 0; i < 8; ++i) pw[i] = pk2(__builtin_amdgcn_exp2f(s[2 * i] - mn), __builtin_amdgcn_exp2f(s[2 * i + 1] - mn));
#pragma unroll
            for (int r = 0; r < 16; ++r) o[r] *= al;
            o = mfma32(v0, (u32x4){pw[0], pw[1], pw[2], pw[3]}, o);
            o = mfma32(v1, (u32x4){pw[4], pw[5], pw[6], pw[7]}, o);
        }
        l = __shfl(o[10], l31);
    }
    const float inv = 1.0f / l;
    char* ot = ws + OFF_OT + (size_t)tt * 36 * 1024;
    st16(ot + h * 1024 + lane * 16, (u32x4){pk2(o[0] * inv, o[1] * inv), pk2(o[2] * inv, o[3] * inv), pk2(o[4] * inv, o[5] * inv), pk2(o[6] * inv, o[7] * inv)});
    if (hh == 0) st4(ot + (32 + (h >> 3)) * 1024 + (((h & 7) >> 2) * 32 + l31) * 16 + (h & 3) * 4, pk2(o[8] * inv, o[9] * inv));
    TS(2, 5);
    }
    int tz = threadIdx.x; asm volatile("" : "+v"(tz));
    const int lane = tz & 63;
    const unsigned voff = lane * 16;
    K4Ctx c;
    c.ldsbase = ldsbase; c.wid = wid; c.voff = voff; c.young = wid >= 4;
    const int mtC = 2 * (xcd & 3) + (j & 1), ftC = 9 * (16 * (xcd >> 2) + (j >> 1));
    c.wsrc = ws + OFF_WGU + (size_t)ftC * K4_SLOT;
    {
        const int tt = 8 * qb + (member >> 2), cg = cgB;
        const int hh = lane >> 5, l31 = lane & 31;
        LAS f32x2* red = (LAS f32x2*)(lds + KB_RED);
        WAIT_VM(0);
        if (lane == 0) flag_set(ws, FL_A + (qb * 8 + wid) * 32 + member);
        if (wid < nT) {
            const int dT = dTB;
            u32x4 xr0, xr1;
            k3_pin<0>(af);
            asm volatile("s_barrier" ::: "memory");
            {
                const u16* xr = x + (size_t)(32 * tt + l31) * D_ + 32 * dT + 8 * hh;
                asm volatile("global_load_dwordx4 %0, %2, off\n\tglobal_load_dwordx4 %1, %2, off offset:32" : "=&v"(xr0), "=&v"(xr1) : "v"(xr) : "memory");
            }
            lds_wait(lds + KB_CNT + 8);
            TS(2, 6);
            u32x4 b[4];
            lptr bp = lds + KB_OT + lane * 16;
#pragma unroll
            for (int k = 0; k < 4; ++k) b[k] = lds128(bp + k * 1024);
            f32x16 acc;
            k3_steps<0>(acc, af, b, bp);
            TS(4, 0);
            asm volatile("s_waitcnt vmcnt(0)" : "+v"(xr0), "+v"(xr1) :: "memory");
            float s1 = 0.f, s2 = 0.f;
            unsigned hp[8];
#pragma unroll
            for (int i = 0; i < 8; ++i) {
                const unsigned xv = i < 4 ? xr0[i] : xr1[i - 4];
                hp[i] = pk2(acc[2 * i] + bflo(xv), acc[2 * i + 1] + bfhi(xv));
                const float a = bflo(hp[i]), bb = bfhi(hp[i]);
                s1 += a + bb; s2 += a * a + bb * bb;
            }
            char* ht = ws + OFF_HT + (size_t)(tt * 36 + 2 * dT) * 1024 + lane * 16;
            st16(ht, (u32x4){hp[0], hp[1], hp[2], hp[3]});
            st16(ht + 1024, (u32x4){hp[4], hp[5], hp[6], hp[7]});
            s1 += xhalf(s1); s2 += xhalf(s2);
            if (hh == 0) red[wid * 32 + l31] = (f32x2){s1, s2};
            asm volatile("s_waitcnt lgkmcnt(0)" ::: "memory");
            unsigned arrived = 0;
            if (lane == 0) arrived = __hip_atomic_fetch_add((LAS unsigned*)(lds + KB_CNT + 4), 1u, __ATOMIC_RELAXED, __HIP_MEMORY_SCOPE_WORKGROUP);
            if (__builtin_amdgcn_readfirstlane(arrived) == (unsigned)(nT - 1) && lane < 32) {
                f32x2 r = red[l31];
#pragma unroll
                for (int i = 1; i < 4; ++i) r += red[i * 32 + l31];
                if (nT == 5) r += red[4 * 32 + l31];
                st8(ws + OFF_STAT + (size_t)(cg * 2048 + 32 * tt + l31) * 8, __builtin_bit_cast(u32x2, r));
            }
            TS(4, 2);
        } else {
            asm volatile("s_barrier" ::: "memory");
            if (wid == 7) {
                flag_wait(ws, FL_A + (qb * 8 + (tt & 7)) * 32 + (lane & 31), 1u);
                TS(3, 2);
                const char* osrc = ws + OFF_OT + (size_t)tt * 36 * 1024;
#pragma unroll
                for (int i = 0; i < 9; ++i) dma4k_dev(voff, osrc + i * 4096, ldsbase + KB_OT + i * 4096);
                if (lane == 0) __hip_atomic_store((LAS unsigned*)(lds + KB_CNT + 12), 1u, __ATOMIC_RELAXED, __HIP_MEMORY_SCOPE_WORKGROUP);
                WAIT_VM(0);
                if (lane == 0) __hip_atomic_store((LAS unsigned*)(lds + KB_CNT + 8), 1u, __ATOMIC_RELAXED, __HIP_MEMORY_SCOPE_WORKGROUP);
            } else if (wid >= 5) {
                flag_wait(ws, FL_PRE + (lane & 15) * 32, 16u);
                const int w5 = wid - 5;
                float cp[3][9];
#pragma unroll
                for (int r = 0; r < 3; ++r) {
                    const int cc = 144 * w5 + 64 * r + lane;
                    const char* p = ws + OFF_CSP + (size_t)ftC * 128;
#pragma unroll
                    for (int i = 0; i < 9; ++i)
                        asm volatile("global_load_dword %0, %1, %2 sc1" : "=v"(cp[r][i]) : "v"((unsigned)(((r < 2 || lane < 16) ? cc : 0) * 4)), "s"(p + (size_t)i * 288 * 128) : "memory");
                }
                lds_wait(lds + KB_CNT + 12);
#pragma unroll
                for (int i = 0; i < 13; ++i) dma4k_dev(voff, c.wsrc + (2 * i + w5) * 4096, ldsbase + (2 * i + w5) * 4096);
                if (w5 == 0) dma4k_dev(voff, c.wsrc + 26 * 4096, ldsbase + 26 * 4096);
                asm volatile("s_waitcnt vmcnt(52)" : "+v"(cp[0][0]), "+v"(cp[0][1]), "+v"(cp[0][2]), "+v"(cp[0][3]), "+v"(cp[0][4]), "+v"(cp[0][5]), "+v"(cp[0][6]), "+v"(cp[0][7]), "+v"(cp[0][8]) :: "memory");
                asm volatile("" : "+v"(cp[1][0]), "+v"(cp[1][1]), "+v"(cp[1][2]), "+v"(cp[1][3]), "+v"(cp[1][4]), "+v"(cp[1][5]), "+v"(cp[1][6]), "+v"(cp[1][7]), "+v"(cp[1][8]));
                asm volatile("" : "+v"(cp[2][0]), "+v"(cp[2][1]), "+v"(cp[2][2]), "+v"(cp[2][3]), "+v"(cp[2][4]), "+v"(cp[2][5]), "+v"(cp[2][6]), "+v"(cp[2][7]), "+v"(cp[2][8]));
                LAS float* csl = (LAS float*)(lds + K4_CS) + 144 * w5;
                float s0 = 0.f, s1 = 0.f, s2 = 0.f;
#pragma unroll
                for (int i = 0; i < 9; ++i) { s0 += cp[0][i]; s1 += cp[1][i]; s2 += cp[2][i]; }
                csl[lane] = s0; csl[64 + lane] = s1;
                if (lane < 16) csl[128 + lane] = s2;
            }
        }
        asm volatile("s_waitcnt vmcnt(0) lgkmcnt(0)\n\ts_barrier" ::: "memory");
        TS(2, 7);
        if (wid == 7) {
            if (lane == 0) flag_set(ws, FL_B + qb * 32 + member);
            flag_wait(ws, FL_B + mtC * 32 + (lane & 31), 1u);
        }
        asm volatile("s_barrier" ::: "memory");
    }
    TS(2, 8);
    const int tt = 8 * mtC + wid;
    const int l15 = lane & 15, kq = lane >> 4;
    c.voffx = (kq >> 1) * 1024 + (kq & 1) * 512 + l15 * 16;
    c.voffa = (kq >> 1) * 512 + l15 * 16 + (kq & 1) * 8;
    c.hsrc = ws + OFF_HT + (size_t)tt * 36 * 1024;
    u32x4 xf[36];
    auto stats = [&]() { dma1k_dev(voff, ws + OFF_STAT + (size_t)((wid >> 1) * 2048 + 256 * mtC + 128 * (wid & 1)) * 8, ldsbase + KB_STAT + wid * 1024); };
    c.adst = ws + OFF_ACT + ((size_t)tt * 288 + ftC) * 1024;
    c.csw = (LAS const float*)(lds + K4_CS) + 4 * kq;
#pragma unroll
    for (int i = 0; i < 4; ++i) { c.slot[i] = lds + i * K4_SLOT + lane * 16; PIN(c.slot[i]); }
    K4Acc accA, accB, accC; K4Epi E;
#define K4_BAR(N) asm volatile("s_waitcnt vmcnt(" #N ")\n\ts_barrier" ::: "memory")
    {
        u32x4 a6[6];
#pragma unroll
        for (int i = 0; i < 6; ++i) a6[i] = lds128(c.slot[i >> 1] + (i & 1) * 1024);
        k4_first<0>(accA, accB, accC, a6, xf, c, stats);
    }
    K4_BAR(0);
    TS(2, 9);
    u32x4 a[K4_NB];
#define K4_PRIME(F) _Pragma("unroll") for (int k = 0; k < K4_NB; ++k) a[k] = lds128(c.slot[(F) & 3] + k * 1024)
    K4_PRIME(3);
#pragma unroll
    for (int u = 0; u < 2; ++u) {
        LAS const f32x2* sp = (LAS const f32x2*)(lds + KB_STAT) + 32 * wid + 16 * u + l15;
        const f32x2 st = sp[0] + sp[256] + sp[512] + sp[768];
        c.m[u] = st[0] * (1.f / 576.f);
        c.V[u] = fmaxf(st[1] * (1.f / 576.f) - c.m[u] * c.m[u], 0.f) + 1e-5f;
        const float rstd = __builtin_amdgcn_rsqf(c.V[u]);
        c.C0[u] = -2.f * 0.7978845608028654f * 1.4426950408889634f * rstd; c.C1[u] = c.C0[u] * 0.044715f * rstd * rstd;
    }
    k4_epi_begin(E, c, 0);
    k4_epi_all<0>(E, accA, c);
    k4_store<0>(E, c, 0); k4_store<1>(E, c, 0);
    SBAR();
    k4_steps<3, 0>(accA, accB, accC, a, xf, E, c);
    K4_BAR(3); K4_PRIME(4);
    k4_steps<4, 0>(accB, accA, accA, a, xf, E, c);
    K4_BAR(6);
    k4_steps<5, 0>(accA, accB, accB, a, xf, E, c);
    K4_BAR(6);
    k4_steps<6, 0>(accB, accA, accA, a, xf, E, c);
    K4_BAR(4);
    k4_steps<7, 0>(accA, accB, accB, a, xf, E, c);
    k4_steps<8, 0>(accB, accA, accA, a, xf, E, c);
    TS(2, 10);
    k4_epi_begin(E, c, 8);
    k4_epi_all<0>(E, accB, c);
    k4_store<0>(E, c, 8); k4_store<1>(E, c, 8);
    TS(2, 11); TR(2, 15);
#undef K4_BAR
#undef K4_PRIME
}

constexpr int K5_STEP = 20 * 1024, K5_SLOT = 2 * K5_STEP, K5_NS = 4;

template <int N>
__device__ __forceinline__ void k5_steps(f32x4 (&acc)[4][6], const u32x4 (&B)[4], const u32x4 (&A)[6], u32x4 (&Bn)[4], u32x4 (&An)[6],
                                         lptr bp, lptr ap) {
    if constexpr (N < 24) {
        constexpr int i = N / 6, jx = N % 6;
        acc[i][jx] = mfma16(A[jx], B[i], acc[i][jx]); PIN(acc[i][jx]);
        if constexpr ((N & 1) == 0 && N / 2 < 10) {
            constexpr int r = N / 2;
            if constexpr (r == 0) Bn[0] = lds128(bp);
            else if constexpr (r <= 6) An[r - 1] = lds128(ap + (r - 1) * 1024);
            else Bn[r - 6] = lds128(bp + ((r - 6) >> 1) * 2048 + ((r - 6) & 1) * 256);
        }
        SBAR();
        k5_steps<N + 1>(acc, B, A, Bn, An, bp, ap);
    }
}

__global__ __launch_bounds__(512, 2) void k5_down(char* __restrict__ ws) {
    __shared__ __attribute__((aligned(16))) char smem[K5_NS * K5_SLOT];
    const int tid = threadIdx.x, lane = tid & 63, wid = __builtin_amdgcn_readfirstlane(tid >> 6);
    const int bx = blockIdx.x;
    const int idx = (bx & 7) * 30 + (bx >> 3);
    const int sp = idx / 48, rem = idx % 48, m4 = rem / 3, dg = rem % 3;
    const int k0 = sp < 2 ? 30 * sp : 60 + 28 * (sp - 2), nst = sp < 2 ? 15 : 14;
    lptr lds = (lptr)smem;
    const unsigned ldsbase = (unsigned)(uintptr_t)lds;
    TS(5, 0); TR(5, 14);
    if (wid >= 4) {
        const int l = wid - 4;
        const unsigned voff = lane * 16;
        const char* asrc = ws + OFF_ACT + ((size_t)(4 * m4 + l) * 288 + 2 * k0) * 1024;
        const char* wsrc = ws + OFF_WDT + ((size_t)(dg * 144 + k0) * 12 + 3 * l) * 1024;
        const unsigned adst = ldsbase + l * 2048, wdst = ldsbase + 8192 + l * 3072;
        auto issue1 = [&](int ks, unsigned dst) {
            asm volatile("s_mov_b32 m0, %2\n\ts_nop 0\n\tglobal_load_lds_dwordx4 %0, %1\n\tglobal_load_lds_dwordx4 %0, %1 offset:1024"
                         :: "v"(voff), "s"(asrc + (size_t)ks * 2048), "s"(adst + dst) : "memory");
            asm volatile("s_mov_b32 m0, %2\n\ts_nop 0\n\tglobal_load_lds_dwordx4 %0, %1\n\tglobal_load_lds_dwordx4 %0, %1 offset:1024\n\tglobal_load_lds_dwordx4 %0, %1 offset:2048"
                         :: "v"(voff), "s"(wsrc + (size_t)ks * 12288), "s"(wdst + dst) : "memory");
        };
        auto issue = [&](int s, int slot) { issue1(2 * s, slot * K5_SLOT); issue1(2 * s + 1, slot * K5_SLOT + K5_STEP); };
        issue(0, 0); issue1(2, K5_SLOT);
        WAIT_VM(5); __builtin_amdgcn_s_barrier();
        issue1(3, K5_SLOT + K5_STEP); issue(2, 2);
        int slot = 3;
        for (int s = 1; s <= nst; ++s) {
            if (s + 1 < nst) WAIT_VM(10); else WAIT_VM(0);
            __builtin_amdgcn_s_barrier();
            if (s + 2 < nst) issue(s + 2, slot);
            slot = (slot + 1) & 3;
        }
        return;
    }
    const int a = wid >> 1, b = wid & 1;
    const int l15 = lane & 15, kq = lane >> 4;
    lptr bp0 = lds + (2 * a) * 2048 + (kq >> 1) * 1024 + (kq & 1) * 512 + l15 * 16, ap0 = lds + 8192 + (6 * b) * 1024 + lane * 16;
    f32x4 acc[4][6] = {};
    u32x4 B0[4], A0[6], B1[4], A1[6];
    __builtin_amdgcn_s_barrier();
    asm volatile("" ::: "memory");
    TS(5, 1);
#pragma unroll
    for (int i = 0; i < 4; ++i) B0[i] = lds128(bp0 + (i >> 1) * 2048 + (i & 1) * 256);
#pragma unroll
    for (int i = 0; i < 6; ++i) A0[i] = lds128(ap0 + i * 1024);
    int slot = 0;
    for (int s = 0; s < nst; ++s) {
        const int nslot = (slot + 1) & 3;
        k5_steps<0>(acc, B0, A0, B1, A1, bp0 + slot * K5_SLOT + K5_STEP, ap0 + slot * K5_SLOT + K5_STEP);
        asm volatile("s_barrier" ::: "memory");
        k5_steps<0>(acc, B1, A1, B0, A0, bp0 + nslot * K5_SLOT, ap0 + nslot * K5_SLOT);
        slot = nslot;
    }
    TS(5, 2);
    {
        char* pbase = ws + OFF_PART + (size_t)sp * (128 * 18 * 1024) + lane * 16;
#pragma unroll
        for (int n = 0; n < 12; ++n) {
            const int i = n / 3, p = n % 3;
            const int t16 = 2 * (4 * m4 + 2 * a + (i >> 1)) + (i & 1), dT = 6 * dg + 3 * b + p;
            const f32x4 v0 = acc[i][2 * p], v1 = acc[i][2 * p + 1];
            st16(pbase + (size_t)(t16 * 18 + dT) * 1024, (u32x4){pk2(v0[0], v0[1]), pk2(v0[2], v0[3]), pk2(v1[0], v1[1]), pk2(v1[2], v1[3])});
        }
    }
    TS(5, 3); TR(5, 15);
}
__global__ __launch_bounds__(256) void k6_reduce(const char* __restrict__ ws, u16* __restrict__ out) {
    const int lane = threadIdx.x & 63, gw = blockIdx.x * 4 + (threadIdx.x >> 6);
    const int t16 = gw / 18, dT = gw % 18, l15 = lane & 15, q = lane >> 4;
    u32x4 w[5];
#pragma unroll
    for (int sp = 0; sp < 5; ++sp) w[sp] = *(const u32x4*)(ws + OFF_PART + (size_t)sp * (128 * 18 * 1024) + (size_t)gw * 1024 + lane * 16);
    const u32x4 h = *(const u32x4*)(ws + OFF_HT + (size_t)((t16 >> 1) * 36 + 2 * dT + (q >> 1)) * 1024 + (q & 1) * 512 + (16 * (t16 & 1) + l15) * 16);
    u32x4 o;
#pragma unroll
    for (int i = 0; i < 4; ++i) {
        float lo = bflo(h[i]), hi = bfhi(h[i]);
        float slo = 0.f, shi = 0.f;
#pragma unroll
        for (int sp = 0; sp < 5; ++sp) { slo += bflo(w[sp][i]); shi += bfhi(w[sp][i]); }
        o[i] = pk2(slo + lo, shi + hi);
    }
    *(u32x4*)(out + (size_t)(16 * t16 + l15) * D_ + 32 * dT + 8 * q) = o;
}

static void launch_one(int k, void* const* d_in, void* d_out, void* d_ws, hipStream_t stream) {
    const u16* x = (const u16*)d_in[0];
    const u16* wqkv = (const u16*)d_in[1];
    const u16* wo = (const u16*)d_in[2];
    const u16* wg = (const u16*)d_in[4];
    const u16* wu = (const u16*)d_in[5];
    const u16* wd = (const u16*)d_in[6];
    char* ws = (char*)d_ws;
    switch (k) {
    case 1: k1_qkv<<<256, 512, 0, stream>>>(x, wqkv, wo, ws); break;
    case 2: kb_fused<<<256, 512, 0, stream>>>(x, wg, wu, wd, ws); break;
    case 3: k5_down<<<240, 512, 0, stream>>>(ws); break;
    case 4: k6_reduce<<<576, 256, 0, stream>>>(ws, (u16*)d_out); break;
    }
}
extern "C" void kernel_launch(void* const* d_in, const int* in_sizes, int n_in, void* d_out, int out_size, void* d_ws, size_t ws_size,
                              hipStream_t stream) {
    for (int k = 1; k <= 4; ++k) launch_one(k, d_in, d_out, d_ws, stream);
}
```

```cpp
#include <hip/hip_runtime.h>
#include <stdint.h>
#include <type_traits>

typedef unsigned short u16;
typedef __attribute__((ext_vector_type(8))) __bf16 bf16x8;
typedef __attribute__((ext_vector_type(2))) __bf16 bf16x2;
typedef __attribute__((ext_vector_type(4))) short s16x4;
typedef __attribute__((ext_vector_type(16))) float f32x16;
typedef __attribute__((ext_vector_type(4))) float f32x4;
typedef __attribute__((ext_vector_type(2))) float f32x2;
typedef __attribute__((ext_vector_type(4))) unsigned u32x4;
typedef __attribute__((ext_vector_type(2))) unsigned u32x2;
#define LAS __attribute__((address_space(3)))
typedef LAS char* lptr;

constexpr int S_ = 2048, D_ = 576, F_ = 4608;
constexpr int QTILE = 1152;
constexpr int KVPAIR = 4736, KVT = 2240, KVTAIL = 4480, KVHEAD = 32 * KVPAIR;
constexpr size_t OFF_QIMG = 0;
constexpr size_t OFF_KV   = OFF_QIMG + (size_t)32 * 64 * QTILE;
constexpr size_t OFF_OT   = OFF_KV + (size_t)32 * KVHEAD;
constexpr size_t OFF_HT   = OFF_OT + (size_t)64 * 36 * 1024;
constexpr size_t OFF_STAT = OFF_HT + (size_t)64 * 36 * 1024;
constexpr size_t OFF_WOT  = OFF_STAT + (size_t)4 * 2048 * 8;
constexpr size_t OFF_WGU  = OFF_WOT + (size_t)18 * 36 * 1024;
constexpr size_t OFF_CSP  = OFF_WGU + (size_t)288 * 36 * 1024;
constexpr size_t OFF_WDT  = OFF_CSP + (size_t)9 * 288 * 32 * 4;
constexpr size_t OFF_ACT  = OFF_WDT + (size_t)18 * 288 * 1024;
constexpr size_t OFF_PART = OFF_ACT + (size_t)64 * 288 * 1024;
constexpr size_t OFF_FLAG = OFF_PART + (size_t)48 * 25 * 12 * 1024;
constexpr int NFLAG = 7168;
constexpr int FL_PRE = 0, FL_B = 512, FL_A = 1024, FL_C = 3072, FL_D = 5120;
constexpr size_t OFF_END  = OFF_FLAG + NFLAG * 4;

__device__ __forceinline__ f32x16 mfma32(u32x4 a, u32x4 b, f32x16 c) {
    return __builtin_amdgcn_mfma_f32_32x32x16_bf16(__builtin_bit_cast(bf16x8, a), __builtin_bit_cast(bf16x8, b), c, 0, 0, 0);
}
__device__ __forceinline__ f32x16 mfma32k8(u32x2 a, u32x2 b, f32x16 c) {
    return __builtin_amdgcn_mfma_f32_32x32x8bf16_1k(__builtin_bit_cast(s16x4, a), __builtin_bit_cast(s16x4, b), c, 0, 0, 0);
}
__device__ __forceinline__ f32x4 mfma16(u32x4 a, u32x4 b, f32x4 c) {
    return __builtin_amdgcn_mfma_f32_16x16x32_bf16(__builtin_bit_cast(bf16x8, a), __builtin_bit_cast(bf16x8, b), c, 0, 0, 0);
}
__device__ __forceinline__ unsigned pk2(float lo, float hi) {
    f32x2 v = {lo, hi};
    return __builtin_bit_cast(unsigned, __builtin_convertvector(v, bf16x2));
}
__device__ __forceinline__ float bflo(unsigned u) { return __uint_as_float(u << 16); }
__device__ __forceinline__ float bfhi(unsigned u) { return __uint_as_float(u & 0xffff0000u); }
__device__ __forceinline__ void dma16(unsigned voff, const void* sbase, unsigned lds) {
    unsigned sv;
    asm volatile("s_mov_b32 %0, m0\n\ts_mov_b32 m0, %3\n\ts_nop 0\n\tglobal_load_lds_dwordx4 %1, %2\n\ts_mov_b32 m0, %0"
                 : "=&s"(sv) : "v"(voff), "s"(sbase), "s"(lds) : "memory");
}
__device__ __forceinline__ void dma4k_dev(unsigned voff, const void* sbase, unsigned lds) {
    asm volatile("s_mov_b32 m0, %2\n\ts_nop 0\n\tglobal_load_lds_dwordx4 %0, %1 sc1\n\tglobal_load_lds_dwordx4 %0, %1 offset:1024 sc1\n\t"
                 "global_load_lds_dwordx4 %0, %1 offset:2048 sc1\n\tglobal_load_lds_dwordx4 %0, %1 offset:3072 sc1"
                 :: "v"(voff), "s"(sbase), "s"(lds) : "memory");
}
__device__ __forceinline__ void dma1k_dev(unsigned voff, const void* sbase, unsigned lds) {
    asm volatile("s_mov_b32 m0, %2\n\ts_nop 0\n\tglobal_load_lds_dwordx4 %0, %1 sc1" :: "v"(voff), "s"(sbase), "s"(lds) : "memory");
}

__device__ __forceinline__ void st16(void* p, u32x4 v) { asm volatile("global_store_dwordx4 %0, %1, off sc1\n\ts_nop 1" :: "v"(p), "v"(v) : "memory"); }
__device__ __forceinline__ void st16f(void* p, f32x4 v) { st16(p, __builtin_bit_cast(u32x4, v)); }
__device__ __forceinline__ void st8(void* p, u32x2 v) { asm volatile("global_store_dwordx2 %0, %1, off sc1" :: "v"(p), "v"(v) : "memory"); }
__device__ __forceinline__ void st4(void* p, unsigned v) { asm volatile("global_store_dword %0, %1, off sc1" :: "v"(p), "v"(v) : "memory"); }

#define GAS __attribute__((address_space(1)))
__device__ __forceinline__ void flag_set(char* ws, int idx, unsigned v = 1u) {
    __hip_atomic_store((GAS unsigned*)(ws + OFF_FLAG) + idx, v, __ATOMIC_RELAXED, __HIP_MEMORY_SCOPE_AGENT);
}
__device__ __forceinline__ void flag_add(char* ws, int idx) {
    __hip_atomic_fetch_add((GAS unsigned*)(ws + OFF_FLAG) + idx, 1u, __ATOMIC_RELAXED, __HIP_MEMORY_SCOPE_AGENT);
}
__device__ __forceinline__ void flag_wait(char* ws, int idx, unsigned want) {
    GAS unsigned* p = (GAS unsigned*)(ws + OFF_FLAG) + idx;
    for (int spins = 0; spins < (1 << 16); ++spins) {
        const unsigned v = __hip_atomic_load(p, __ATOMIC_RELAXED, __HIP_MEMORY_SCOPE_AGENT);
        if (__all(v >= want)) break;
        __builtin_amdgcn_s_sleep(1);
    }
    asm volatile("" ::: "memory");
}
__device__ __forceinline__ void lds_wait(__attribute__((address_space(3))) char* p) {
    for (int spins = 0; spins < (1 << 20); ++spins) {
        if (__hip_atomic_load((__attribute__((address_space(3))) unsigned*)p, __ATOMIC_RELAXED, __HIP_MEMORY_SCOPE_WORKGROUP) != 0u) break;
        __builtin_amdgcn_s_sleep(1);
    }
    asm volatile("" ::: "memory");
}
#define WAIT_VM(n) asm volatile("s_waitcnt vmcnt(" #n ")" ::: "memory")
#define SBAR() __builtin_amdgcn_sched_barrier(0)
#define PIN(x) asm volatile("" : "+v"(x))
__device__ __forceinline__ u32x4 lds128(lptr p) { return *(LAS const u32x4*)p; }
__device__ __forceinline__ u32x2 lds64(lptr p) { return *(LAS const u32x2*)p; }
typedef short v4i16_t __attribute__((ext_vector_type(4)));
__device__ __forceinline__ u32x2 ldstr(lptr p) { return __builtin_bit_cast(u32x2, __builtin_amdgcn_ds_read_tr16_b64_v4i16((LAS v4i16_t*)p)); }
__device__ __forceinline__ float xhalf(float v) {
    return __shfl_xor(v, 32);
}
__device__ __forceinline__ int crow(int r, int hh) { return (r & 3) + 8 * (r >> 2) + 4 * hh; }
__device__ __forceinline__ size_t kvtile(int h, int tt) { return (size_t)h * KVHEAD + (tt >> 1) * KVPAIR + (tt & 1) * KVT; }

__device__ __forceinline__ int phi32(int rho) { const int r = (rho & 3) + 4 * (rho >> 3), hc = (rho >> 2) & 1; return 16 * (r >> 3) + 8 * hc + (r & 7); }
struct SideW { const u16 *wo, *wg, *wu, *wd; char* ws; };
struct SideJob {
    int type;
    const char* src[2];
    int pstep[4];
    unsigned voff;
    char* dst[2]; int dstep[4];
    int raddr[4];
    float* cs;
};
__device__ __forceinline__ SideJob side_job(const SideW& w, int item, int lane) {
    SideJob g;
    const int q4 = (lane >> 2) & 3, p4 = lane & 3, grp = lane >> 4;
    g.cs = nullptr; g.type = 3; g.voff = 0;
    int rs = D_ * 2;
    if (item < 1296) {
        const int U = item / 18, mat = (item % 18) / 9, sp = item % 9;
        g.type = 0; rs = F_ * 2;
        g.src[0] = (const char*)(mat ? w.wu : w.wg) + 128 * U + (size_t)(64 * sp) * rs; g.src[1] = g.src[0] + 32 * rs;
        g.dst[0] = w.ws + OFF_WGU + (size_t)((4 * U * 18 + 2 * sp) * 2 + mat) * 1024; g.dst[1] = g.dst[0] + 2048;
        g.cs = (float*)(w.ws + OFF_CSP) + (size_t)(sp * 288 + 4 * U) * 32 + mat * 16;
#pragma unroll
        for (int t = 0; t < 4; ++t) { g.pstep[t] = 8 * t * rs; g.dstep[t] = t * 36 * 1024; g.raddr[t] = (8 * grp + q4) * 128 + 32 * t + 8 * p4; }
        g.voff = (lane >> 3) * rs + (lane & 7) * 16;
    } else if (item < 1944) {
        const int it = item - 1296, V = it / 72, sp = it % 72;
        g.type = 1;
        g.src[0] = (const char*)w.wd + 128 * V + (size_t)(64 * sp) * rs; g.src[1] = g.src[0] + 32 * rs;
        g.dst[0] = w.ws + OFF_WDT + (size_t)(((V / 3) * 144 + 2 * sp) * 12 + (V % 3) * 4) * 1024; g.dst[1] = g.dst[0] + 12 * 1024;
#pragma unroll
        for (int t = 0; t < 4; ++t) { g.pstep[t] = 8 * t * rs; g.dstep[t] = t * 1024; g.raddr[t] = (8 * grp + q4) * 128 + 64 * (t >> 1) + 16 * p4 + 8 * (t & 1); }
        g.voff = (lane >> 3) * rs + (lane & 7) * 16;
    } else {
        const int it = item < 2025 ? item - 1944 : 0, V = it / 9, kq = it % 9;
        const bool mainp = kq < 8;
        const int jj = lane >> 3;
        if (item < 2025) g.type = 2;
#pragma unroll
        for (int b = 0; b < 2; ++b) {
            const int ks = 4 * kq + 2 * b;
            g.src[b] = (const char*)w.wo + 128 * V + (size_t)(mainp ? 18 * ks : 144 * (ks - 32)) * rs;
            g.dst[b] = w.ws + OFF_WOT + (size_t)(2 * V * 36 + ks) * 1024;
        }
#pragma unroll
        for (int t = 0; t < 4; ++t) {
            g.pstep[t] = (mainp ? 20 * (t >> 1) + 4 * (t & 1) : 144 * (t >> 1) + 72 * (t & 1)) * rs;
            g.dstep[t] = (t >> 1) * 1024 + (t & 1) * 36 * 1024;
            g.raddr[t] = (16 * (t >> 1) + 8 * (grp >> 1) + q4) * 128 + 64 * (t & 1) + 2 * phi32(16 * (grp & 1) + 4 * p4);
        }
        g.voff = (mainp ? 8 * (jj >> 2) + (jj & 3) : 18 * (jj >> 1) + (((jj >> 1) & 1) ? (jj & 1) : 16 + (jj & 1))) * rs + (lane & 7) * 16;
    }
    return g;
}
__device__ __forceinline__ void side_fetch(const SideJob& g, int b, unsigned ldsbuf) {
    if (g.type != 3) {
#pragma unroll
        for (int p = 0; p < 4; ++p) dma16(g.voff, g.src[b] + g.pstep[p], ldsbuf + p * 1024);
    }
}
__device__ __forceinline__ void side_finish(const SideJob& g, int b, lptr buf, f32x4 (&cs)[4], int lane) {
    if (g.type == 3) return;
    u32x4 v[4];
#pragma unroll
    for (int t = 0; t < 4; ++t) {
        const u32x2 lo = ldstr(buf + g.raddr[t]), hi = ldstr(buf + g.raddr[t] + 512);
        v[t] = (u32x4){lo[0], lo[1], hi[0], hi[1]};
    }
#pragma unroll
    for (int t = 0; t < 4; ++t) st16(g.dst[b] + g.dstep[t] + lane * 16, v[t]);
    if (g.type == 0) {
        const u32x4 ones = {0x3F803F80u, 0x3F803F80u, 0x3F803F80u, 0x3F803F80u};
#pragma unroll
        for (int t = 0; t < 4; ++t) cs[t] = mfma16(v[t], ones, cs[t]);
        asm volatile("s_nop 7\n\ts_nop 1" : "+v"(cs[0]), "+v"(cs[1]), "+v"(cs[2]), "+v"(cs[3]));
        if (b == 1 && (lane & 15) == 0) {
#pragma unroll
            for (int t = 0; t < 4; ++t) st16f(g.cs + t * 32 + (lane >> 4) * 4, cs[t]);
        }
    }
}

constexpr int K1_BUF = 32768, K1_NB = 4, K1_ST = K1_NB * K1_BUF;
constexpr float QSCALE = 0.23570226039551584f * 1.4426950408889634f;
__device__ const unsigned k1_ones[4] = {0x3F803F80u, 0x3F803F80u, 0x3F803F80u, 0x3F803F80u};

constexpr int K1_RAW = K1_ST, K1_CS = K1_ST + 1024, K1_PV = K1_ST + 1536, K1_SIDE = K1_PV + 8 * 256, K1_TOUCH = K1_SIDE + 8192, K1_LDS = K1_TOUCH + 256;
__device__ __forceinline__ void k1_finish(const f32x16& acc, int f, int t, bool isV, char* __restrict__ ws, lptr lds, int mt, int g, int wid, int lane) {
    const int hh = lane >> 5, l31 = lane & 31, tt = 4 * mt + t;
    char* qimg = ws + OFF_QIMG; char* kv = ws + OFF_KV;
    LAS const float* cs = (LAS const float*)(lds + K1_CS);
    const f32x2 sv = *(LAS const f32x2*)(lds + K1_RAW + (32 * t + l31) * 8);
    const float mean = sv[0] * (1.f / 576.f);
    const float rstd = __builtin_amdgcn_rsqf(fmaxf(sv[1] * (1.f / 576.f) - mean * mean, 0.f) + 1e-5f), mrs = mean * rstd;
    auto pair = [](const f32x16& a, int r) { return (f32x2){a[r], a[r + 1]}; };
    unsigned pk[8];
    if (isV) {
        LAS float* pv = (LAS float*)(lds + K1_PV + wid * 256);
        if (hh == 0) { pv[l31] = rstd; pv[32 + l31] = mrs; }
        const float c = -cs[32 * f + l31];
        asm volatile("s_waitcnt lgkmcnt(0)" ::: "memory");
#pragma unroll
        for (int q = 0; q < 4; ++q) {
            const f32x4 rs = *(LAS const f32x4*)(pv + 8 * q + 4 * hh), mr = *(LAS const f32x4*)(pv + 32 + 8 * q + 4 * hh);
            const f32x2 v0 = __builtin_elementwise_fma(pair(acc, 4 * q), (f32x2){rs[0], rs[1]}, (f32x2){mr[0], mr[1]} * (f32x2){c, c});
            const f32x2 v1 = __builtin_elementwise_fma(pair(acc, 4 * q + 2), (f32x2){rs[2], rs[3]}, (f32x2){mr[2], mr[3]} * (f32x2){c, c});
            pk[2 * q] = pk2(v0[0], v0[1]); pk[2 * q + 1] = pk2(v1[0], v1[1]);
        }
        asm volatile("s_waitcnt lgkmcnt(0)" ::: "memory");
        int h, d;
        if (f < 3) { h = 2 * ((3 * g + f) & 15) + (l31 >> 4); d = l31 & 15; }
        else if (l31 < 12) { h = 2 * ((3 * g + (l31 >> 2)) & 15) + ((l31 >> 1) & 1); d = 16 + (l31 & 1); }
        else {
            const int i = l31 < 18 ? l31 - 12 : 0;
            h = 2 * ((3 * g + (i >> 1)) & 15) + (i & 1); d = 18;
#pragma unroll
            for (int i2 = 0; i2 < 8; ++i2) pk[i2] = 0x3F803F80u;
        }
        if (f < 3 || l31 < 18) {
            char* dst = kv + kvtile(h, tt) + 1024 + hh * 304 + d * 16;
            st16(dst, (u32x4){pk[0], pk[1], pk[2], pk[3]});
            st16(dst + 608, (u32x4){pk[4], pk[5], pk[6], pk[7]});
        }
        return;
    }
    const int P0 = 3 * g + (f < 3 ? f : 0), sec0 = P0 >> 4;
    const float sc = (f < 3 && sec0 == 0) ? QSCALE : 1.f;
    const float rs = rstd * sc, nmr = -mrs * sc;
    f32x2 v[8];
#pragma unroll
    for (int q = 0; q < 4; ++q) {
        const f32x4 cq = *(LAS const f32x4*)(cs + 32 * f + 8 * q + 4 * hh);
        v[2 * q] = __builtin_elementwise_fma(pair(acc, 4 * q), (f32x2){rs, rs}, (f32x2){cq[0], cq[1]} * (f32x2){nmr, nmr});
        v[2 * q + 1] = __builtin_elementwise_fma(pair(acc, 4 * q + 2), (f32x2){rs, rs}, (f32x2){cq[2], cq[3]} * (f32x2){nmr, nmr});
    }
    if (f < 3) {
        const int pp = P0 & 15;
#pragma unroll
        for (int i = 0; i < 8; ++i) pk[i] = pk2(v[i][0], v[i][1]);
        char* base = sec0 == 0 ? qimg + (size_t)(2 * pp * 64 + tt) * QTILE : kv + kvtile(2 * pp, tt);
        const size_t hs = sec0 == 0 ? (size_t)64 * QTILE : (size_t)KVHEAD;
        st16(base + lane * 16, (u32x4){pk[0], pk[1], pk[2], pk[3]});
        st16(base + hs + lane * 16, (u32x4){pk[4], pk[5], pk[6], pk[7]});
    } else {
#pragma unroll
        for (int u = 0; u < 2; ++u) {
            const int p = 2 * u + hh, P = 3 * g + p, sec = P >> 4, pp = P & 15;
            const float sq = sec == 0 ? QSCALE : 1.f;
#pragma unroll
            for (int o = 0; o < 2; ++o) {
                const int h = 2 * pp + o;
                const f32x2 e = v[2 * u + o];
                const unsigned pq = pk2(e[0] * sq, e[1] * sq);
                const size_t offq = OFF_QIMG + (size_t)(h * 64 + tt) * QTILE + 1024 + l31 * 4;
                const size_t offk = OFF_KV + (size_t)h * KVHEAD + (tt >> 1) * KVPAIR + KVTAIL + l31 * 8 + (tt & 1) * 4;
                if (p < 3 && sec < 2) st4(ws + (sec == 0 ? offq : offk), pq);
                if (g == 10 && u == 1 && hh == 0) {
                    const int off = (2 * (l31 >> 4) + ((l31 >> 2) & 1)) * 304 + (((l31 >> 3) & 1) * 4 + (l31 & 3)) * 2;
                    char* dst = kv + kvtile(h, tt) + 1024 + off;
                    *(u16*)(dst + 16 * 16) = (u16)(pq & 0xffff);
                    *(u16*)(dst + 17 * 16) = (u16)(pq >> 16);
                    *(u16*)(dst + 18 * 16) = (u16)0x3F80;
                }
            }
        }
    }
}

struct K1Frag { u32x4 x0, x1, w0, w1; };
template <bool V0, bool V1, bool T0, bool T1>
__device__ __forceinline__ void k1_body(char* __restrict__ ws, lptr lds, int mt, int g, int wid, int lane) {
    const int a = wid >> 1, b = wid & 1;
    const int hh = lane >> 5, l31 = lane & 31;
    const int t0 = 2 * b + a, t1 = 2 * b + (1 - a);
    const int f0 = 2 * a + b, f1 = 2 * a + (1 - b);
    const int sw = (l31 >> 1) & 7;
    const int xc = (hh ^ sw) << 4;
    int xa0[4], xa1[4];
#pragma unroll
    for (int s = 0; s < 4; ++s) { xa0[s] = (32 * t0 + l31) * 128 + (xc ^ (s << 5)); xa1[s] = (32 * t1 + l31) * 128 + (xc ^ (s << 5)); }
    const int q4 = (lane >> 2) & 3, p4 = lane & 3, cg = (lane >> 4) & 1;
    auto waddr = [&](int f) {
        const int c = f < 3 ? 4 * f + 2 * cg + (p4 >> 1) : (cg ? 15 : 12 + p4);
        const int sub = f < 3 ? 8 * (p4 & 1) : 0;
        return 16384 + (8 * hh + q4) * 256 + ((c ^ (q4 << 2)) << 4) + sub;
    };
    const int wa0 = waddr(f0), wa1 = waddr(f1);

    f32x16 acc00 = {}, acc01 = {}, acc10 = {}, acc11 = {};
    float sqa = 0.f, sqb = 0.f;
    f32x4 accC = {};
    const unsigned pick = ((lane & 15) == ((lane >> 4) & 1)) ? 0x3F803F80u : 0u;
    const u32x4 sumA = {pick, pick, pick, pick};
    auto step = [&](K1Frag& C, K1Frag& N, lptr nb, int nks) {
        u32x2 lo, hi;
        auto sq = [&](int i) {
            const float lo = bflo(C.x0[i]), hi = bfhi(C.x0[i]);
            sqa = __builtin_fmaf(lo, lo, sqa); sqb = __builtin_fmaf(hi, hi, sqb);
            PIN(sqa); PIN(sqb);
        };
        acc00 = V0 ? mfma32(C.x0, C.w0, acc00) : mfma32(C.w0, C.x0, acc00); PIN(acc00); SBAR();
        N.x0 = lds128(nb + xa0[nks]);
        lo = ldstr(nb + wa0 + nks * 4096); sq(0); SBAR();
        acc01 = V0 ? mfma32(C.x1, C.w0, acc01) : mfma32(C.w0, C.x1, acc01); PIN(acc01); SBAR();
        hi = ldstr(nb + wa0 + nks * 4096 + 1024); sq(1); SBAR();
        acc10 = V1 ? mfma32(C.x0, C.w1, acc10) : mfma32(C.w1, C.x0, acc10); PIN(acc10); SBAR();
        N.x1 = lds128(nb + xa1[nks]); sq(2); SBAR();
        accC = mfma16(sumA, C.w0, accC); PIN(accC); SBAR();
        N.w0 = (u32x4){lo[0], lo[1], hi[0], hi[1]};
        lo = ldstr(nb + wa1 + nks * 4096); SBAR();
        acc11 = V1 ? mfma32(C.x1, C.w1, acc11) : mfma32(C.w1, C.x1, acc11); PIN(acc11); SBAR();
        hi = ldstr(nb + wa1 + nks * 4096 + 1024); sq(3);
        N.w1 = (u32x4){lo[0], lo[1], hi[0], hi[1]};
        SBAR();
    };
    K1Frag F0, F1, F2, F3;
    auto rdall = [&](K1Frag& N, int ks) {
        N.x0 = lds128(lds + xa0[ks]); N.x1 = lds128(lds + xa1[ks]);
        const u32x2 a0 = ldstr(lds + wa0 + ks * 4096), a1 = ldstr(lds + wa0 + ks * 4096 + 1024), b0 = ldstr(lds + wa1 + ks * 4096), b1 = ldstr(lds + wa1 + ks * 4096 + 1024);
        N.w0 = (u32x4){a0[0], a0[1], a1[0], a1[1]}; N.w1 = (u32x4){b0[0], b0[1], b1[0], b1[1]};
    };
    asm volatile("s_barrier" ::: "memory");
    rdall(F0, 0); rdall(F1, 1);
    for (int s = 0; s < 9; ++s) {
        lptr buf = lds + (s & 3) * K1_BUF, nbuf = lds + ((s + 1) & 3) * K1_BUF;
        step(F0, F2, buf, 2);
        step(F1, F3, buf, 3);
        asm volatile("s_barrier" ::: "memory");
        step(F2, F0, nbuf, 0);
        step(F3, F1, nbuf, 1);
    }
    {
        LAS float* st = (LAS float*)(lds + K1_RAW);
        LAS float* cs = (LAS float*)(lds + K1_CS);
        LAS f32x4* ho = (LAS f32x4*)(lds + wid * 8192) + lane;
#pragma unroll
        for (int q = 0; q < 4; ++q) {
            ho[q * 64] = (f32x4){acc10[4 * q], acc10[4 * q + 1], acc10[4 * q + 2], acc10[4 * q + 3]};
            ho[(4 + q) * 64] = (f32x4){acc11[4 * q], acc11[4 * q + 1], acc11[4 * q + 2], acc11[4 * q + 3]};
        }
        const float dg = sqa + sqb + xhalf(sqa + sqb);
        if (hh == 0) st[(32 * t0 + l31) * 2 + 1] = dg;
        if (lane < 16) { cs[32 * f0 + lane] = accC[0]; cs[32 * f0 + 16 + lane] = accC[1]; }
        auto rowsum = [&](const f32x16& acc, int t, bool isV) {
            if (!isV) { if (hh == 1) st[(32 * t + l31) * 2] = acc[4]; }
            else if (l31 == 12) {
#pragma unroll
                for (int r = 0; r < 16; ++r) st[(32 * t + crow(r, hh)) * 2] = acc[r];
            }
        };
        if (T0) { rowsum(acc00, t0, V0); rowsum(acc01, t1, V0); }
        if (T1) { rowsum(acc10, t0, V1); rowsum(acc11, t1, V1); }
    }
    asm volatile("s_waitcnt lgkmcnt(0)\n\ts_barrier" ::: "memory");
    k1_finish(acc00, f0, t0, V0, ws, lds, mt, g, wid, lane);
    k1_finish(acc01, f0, t1, V0, ws, lds, mt, g, wid, lane);
}

__global__ __launch_bounds__(512, 2) void k1_qkv(const u16* __restrict__ x, const u16* __restrict__ w, const u16* __restrict__ wo, const u16* __restrict__ wg,
                                                 const u16* __restrict__ wu, const u16* __restrict__ wd, const u16* __restrict__ out, char* __restrict__ ws) {
    __shared__ __attribute__((aligned(16))) char smem[K1_LDS];
    const int lane = threadIdx.x & 63, wid = __builtin_amdgcn_readfirstlane(threadIdx.x >> 6);
    const int bx = blockIdx.x, xcd = bx & 7, j = bx >> 3;
    const int mt = (xcd >> 1) * 4 + (j & 3), g = (xcd & 1) * 8 + (j >> 2);
    lptr lds = (lptr)smem;
    if (wid == 0 && j < 2) {
        constexpr size_t PG = (size_t)2 << 20;
        constexpr int NWS = (int)((OFF_END + PG - 1) / PG) + 1;
        const char* p;
        int i = lane;
        if (j == 0) { const size_t o = (size_t)i * PG; p = ws + (o < OFF_END ? o : OFF_END - 4); }
        else if (i < 3) { const size_t o = (size_t)i * PG; p = (const char*)out + (o < (size_t)S_ * D_ * 2 ? o : (size_t)S_ * D_ * 2 - 4); }
        else if (i < 15) { const int m = (i - 3) >> 2; const size_t o = (size_t)((i - 3) & 3) * PG, sz = (size_t)D_ * F_ * 2;
                           p = (const char*)(m == 0 ? wg : m == 1 ? wu : wd) + (o < sz ? o : sz - 4); }
        else if (i < 17) p = (const char*)x + (size_t)(i - 15) * ((size_t)S_ * D_ * 2 - 4);
        else if (i < 19) p = (const char*)w + (size_t)(i - 17) * ((size_t)D_ * 3 * D_ * 2 - 4);
        else p = (const char*)wo;
        static_assert(NWS <= 64, "");
        asm volatile("s_mov_b32 m0, %1\n\ts_nop 0\n\tglobal_load_lds_dword %0, off" :: "v"(p), "s"((unsigned)(uintptr_t)lds + K1_TOUCH) : "memory");
    }
    if (bx < NFLAG / 512) ((unsigned*)(ws + OFF_FLAG))[bx * 512 + threadIdx.x] = 0u;
    if (wid >= 4) {
        const int l = wid - 4;
        const unsigned ldsbase = (unsigned)(uintptr_t)lds;
        unsigned xoff[4];
        const char* wp[4];
        int wstep[4];
#pragma unroll
        for (int i = 0; i < 4; ++i) {
            const int p = 4 * l + i;
            const int row = 8 * p + (lane >> 3), ch = (lane & 7) ^ ((row >> 1) & 7);
            xoff[i] = row * 1152 + ch * 16;
            const int kr = 4 * p + (lane >> 4), c = (lane & 15) ^ ((kr & 3) << 2);
            const int cs = c & 3;
            const int byte = c < 12 ? 72 * (c >> 2) + (cs < 2 ? 16 * cs : 40 + 16 * (cs - 2)) : 72 * (c - 12) + 32;
            wp[i] = c < 15 ? (const char*)w + g * 216 + kr * 3456 + byte : (const char*)k1_ones;
            wstep[i] = c < 15 ? 64 * 3456 : 0;
        }
        const char* xb = (const char*)x + (size_t)mt * 128 * 1152;
        auto issue = [&](int s) {
            const unsigned dst = ldsbase + (s & 3) * K1_BUF + l * 4096;
#pragma unroll
            for (int i = 0; i < 4; ++i) {
                dma16(xoff[i], xb + s * 128, dst + i * 1024);
                asm volatile("s_mov_b32 m0, %1\n\ts_nop 0\n\tglobal_load_lds_dwordx4 %0, off" :: "v"(wp[i]), "s"(dst + 16384 + i * 1024) : "memory");
                wp[i] += wstep[i];
            }
        };
        issue(0); issue(1);
        SideJob job = {};
        job.type = 3;
        if (l == 3 && bx < 81) { job = side_job(SideW{wo, wo, wo, wo, ws}, 1944 + bx, lane); asm volatile("; wo item"); }
        WAIT_VM(8);
        asm volatile("s_barrier ; #0" ::: "memory");
        issue(2);
        for (int s = 1; s < 7; ++s) {
            WAIT_VM(8);
            __builtin_amdgcn_s_barrier();
            issue(s + 2);
        }
        side_fetch(job, 0, ldsbase + K1_SIDE); side_fetch(job, 1, ldsbase + K1_SIDE + 4096);
        if (job.type != 3) WAIT_VM(16); else WAIT_VM(8);
        asm volatile("s_barrier ; #7" ::: "memory");
        if (job.type != 3) WAIT_VM(8); else WAIT_VM(0);
        asm volatile("s_barrier ; #8" ::: "memory");
        __builtin_amdgcn_s_barrier();
        WAIT_VM(0);
        asm volatile("s_barrier" ::: "memory");
        {
            const int a = l >> 1, b = l & 1, t0 = 2 * b + a, t1 = 2 * b + (1 - a), f1 = 2 * a + (1 - b);
            const bool isV = f1 < 3 ? 3 * g + f1 >= 32 : 3 * g >= 32;
            LAS const f32x4* ho = (LAS const f32x4*)(lds + l * 8192) + lane;
#pragma unroll
            for (int i = 0; i < 2; ++i) {
                f32x16 acc;
#pragma unroll
                for (int q = 0; q < 4; ++q) { const f32x4 v = ho[(4 * i + q) * 64]; acc[4 * q] = v[0]; acc[4 * q + 1] = v[1]; acc[4 * q + 2] = v[2]; acc[4 * q + 3] = v[3]; }
                k1_finish(acc, f1, i == 0 ? t0 : t1, isV, ws, lds, mt, g, wid, lane);
            }
        }
        if (job.type != 3) {
            f32x4 cs[4] = {};
            side_finish(job, 0, lds + K1_SIDE, cs, lane); side_finish(job, 1, lds + K1_SIDE + 4096, cs, lane);
        }
        return;
    }
    const int a = wid >> 1, b = wid & 1;
    const int f0 = 2 * a + b;
    const bool vA = 3 * g >= 32, vB = 3 * g + 2 >= 32;
    if (a == 0) { if (vA) k1_body<true, true, false, false>(ws, lds, mt, g, wid, lane); else k1_body<false, false, false, false>(ws, lds, mt, g, wid, lane); }
    else if (f0 == 2) {
        if (vA) k1_body<true, true, false, true>(ws, lds, mt, g, wid, lane);
        else if (vB) k1_body<true, false, false, true>(ws, lds, mt, g, wid, lane);
        else k1_body<false, false, false, true>(ws, lds, mt, g, wid, lane);
    } else {
        if (vA) k1_body<true, true, true, false>(ws, lds, mt, g, wid, lane);
        else if (vB) k1_body<false, true, true, false>(ws, lds, mt, g, wid, lane);
        else k1_body<false, false, true, false>(ws, lds, mt, g, wid, lane);
    }
}

struct K2Regs { u32x4 km, v0, v1; u32x2 kt; float e0, e1; };
struct NoHook { template <class T> __device__ __forceinline__ void operator()(T) const {} };
template <int N> using IC = std::integral_constant<int, N>;
template <bool ODD, bool QKN, bool LDKN, bool LDVN, class H>
__device__ __forceinline__ void k2_step(f32x16& SC, f32x16& SN, f32x16& o, K2Regs& R, const u32x4& qm, const u32x2& qtA, const u32x2& qtB,
                                        lptr kp, lptr ktp, lptr vp, H&& hook) {
    float e[16]; unsigned pw[8];
#define EX(i) do { e[i] = __builtin_amdgcn_exp2f(SC[i]); PIN(e[i]); } while (0)
#define CV(i) do { pw[i] = pk2(e[2 * (i)], e[2 * (i) + 1]); PIN(pw[i]); } while (0)
    e[0] = R.e0; e[1] = R.e1;
    EX(2); EX(3); EX(4); EX(5);
    SBAR();
    if constexpr (QKN) { SN = mfma32k8(R.kt, ODD ? qtA : qtB, (f32x16){}); PIN(SN); SBAR(); }
    hook(IC<0>{});
    EX(6); EX(7); CV(0); CV(1); CV(2); CV(3);
    SBAR();
    if constexpr (QKN) { SN = mfma32(R.km, qm, SN); PIN(SN); SBAR(); }
    if constexpr (LDKN) {
        R.km = lds128(kp + KVPAIR + (ODD ? KVT : 0));
        if constexpr (!ODD) R.kt = lds64(ktp + KVPAIR);
    }
    EX(8); EX(9); EX(10); EX(11);
    SBAR();
    asm volatile("" :: "v"(R.v0), "v"(R.v1));
    o = mfma32(R.v0, (u32x4){pw[0], pw[1], pw[2], pw[3]}, o); PIN(o); SBAR();
    hook(IC<1>{});
    EX(12); EX(13); EX(14); EX(15); CV(4); CV(5); CV(6); CV(7);
    SBAR();
    if constexpr (QKN) { R.e0 = __builtin_amdgcn_exp2f(SN[0]); PIN(R.e0); R.e1 = __builtin_amdgcn_exp2f(SN[1]); PIN(R.e1); }
    else { asm volatile("s_nop 1"); }
    SBAR();
    o = mfma32(R.v1, (u32x4){pw[4], pw[5], pw[6], pw[7]}, o); PIN(o); SBAR();
    if constexpr (LDVN) { R.v0 = lds128(vp + (ODD ? KVPAIR : KVT)); R.v1 = lds128(vp + (ODD ? KVPAIR : KVT) + 608); }
    SBAR();
#undef EX
#undef CV
}

template <int I>
__device__ __forceinline__ void k2_piece(int c, int wid, unsigned voff, const char* kvsrc, unsigned ldsbase) {
    int off = 40960 + 32768 * c + 4096 * wid;
    off = off + 4096 <= KVHEAD ? off : KVHEAD - 4096;
    if constexpr (I == 0) asm volatile("s_mov_b32 m0, %2\n\ts_nop 0\n\tglobal_load_lds_dwordx4 %0, %1" :: "v"(voff), "s"(kvsrc + off), "s"(ldsbase + off) : "memory");
    else asm volatile("global_load_lds_dwordx4 %0, %1 offset:%2" :: "v"(voff), "s"(kvsrc + off), "i"(I * 1024) : "memory");
}
template <int K>
__device__ __forceinline__ void k3_steps(f32x16& acc, u32x4 (&af)[36], u32x4 (&b)[4], lptr bp) {
    if constexpr (K < 36) {
        acc = mfma32(af[K], b[K & 3], K == 0 ? (f32x16){} : acc); PIN(acc);
        if constexpr (K + 4 < 36) b[K & 3] = lds128(bp + (K + 4) * 1024);
        SBAR();
        k3_steps<K + 1>(acc, af, b, bp);
    }
}
template <int K>
__device__ __forceinline__ void k3_pin(u32x4 (&af)[36]) { if constexpr (K < 36) { PIN(af[K]); k3_pin<K + 1>(af); } }
template <int A>
__device__ __forceinline__ void k3_aload(u32x4 (&af)[36], const char* wsrc, unsigned voff) {
    asm volatile("global_load_dwordx4 %0, %1, %2 offset:%3" : "=v"(af[A]) : "v"(voff), "s"(wsrc + (A / 4) * 4096), "i"((A & 3) * 1024) : "memory");
}

constexpr int K4_SLOT = 36 * 1024, K4_CS = 4 * K4_SLOT, K4_NB = 4;
constexpr int K4_XD = 12;

struct K4Acc { f32x4 g[2], u[2]; };
struct K4Epi { float g[8], u[8], e[8]; unsigned pk[4]; f32x4 cg, cu; };
constexpr int K4_NCH = 8, K4_NOP = 11;
struct K4Ctx {
    lptr slot[4]; LAS const float* csw; float m[2], C0[2], C1[2], V[2];
    const char* wsrc; unsigned ldsbase; int wid; unsigned voff, voffx, voffa; const char* hsrc; char* adst;
};
template <int R, int OP>
__device__ __forceinline__ void k4_epi_op(K4Epi& E, const K4Acc& acc, const K4Ctx& c) {
    constexpr int U = R >> 2, Q = R & 3;
    if constexpr (OP == 0) { E.g[R] = __builtin_fmaf(-c.m[U], E.cg[Q], acc.g[U][Q]); PIN(E.g[R]); }
    else if constexpr (OP == 1) { E.u[R] = __builtin_fmaf(-c.m[U], E.cu[Q], acc.u[U][Q]); PIN(E.u[R]); }
    else if constexpr (OP == 2) { E.e[R] = E.g[R] * E.g[R]; PIN(E.e[R]); }
    else if constexpr (OP == 3) { E.e[R] = __builtin_fmaf(E.e[R], c.C1[U], c.C0[U]); PIN(E.e[R]); }
    else if constexpr (OP == 4) { E.e[R] = E.g[R] * E.e[R]; PIN(E.e[R]); }
    else if constexpr (OP == 5) { E.e[R] = __builtin_amdgcn_exp2f(E.e[R]); PIN(E.e[R]); }
    else if constexpr (OP == 6) { E.e[R] = __builtin_fmaf(E.e[R], c.V[U], c.V[U]); PIN(E.e[R]); }
    else if constexpr (OP == 7) { E.e[R] = __builtin_amdgcn_rcpf(E.e[R]); PIN(E.e[R]); }
    else if constexpr (OP == 8) { E.u[R] = E.g[R] * E.u[R]; PIN(E.u[R]); }
    else if constexpr (OP == 9) { E.u[R] = E.u[R] * E.e[R]; PIN(E.u[R]); }
    else if constexpr (R & 1) { E.pk[R >> 1] = pk2(E.u[R - 1], E.u[R]); PIN(E.pk[R >> 1]); }
}
template <int M, int START, int CSP, int OSP, int R>
__device__ __forceinline__ void k4_epi_slot(K4Epi& E, const K4Acc& acc, const K4Ctx& c) {
    if constexpr (R < K4_NCH) {
        constexpr int d = M - (START + CSP * R);
        if constexpr (d >= 0 && d <= (K4_NOP - 1) * OSP && d % OSP == 0) k4_epi_op<R, d / OSP>(E, acc, c);
        k4_epi_slot<M, START, CSP, OSP, R + 1>(E, acc, c);
    }
}
__device__ __forceinline__ void k4_epi_begin(K4Epi& E, const K4Ctx& c, int f) {
    LAS const float* cs = c.csw + f * 32;
    E.cg = *(LAS const f32x4*)(cs); E.cu = *(LAS const f32x4*)(cs + 16);
}
template <int U>
__device__ __forceinline__ void k4_store(const K4Epi& E, const K4Ctx& c, int f) {
    st8(c.adst + (size_t)f * 1024 + U * 256 + c.voffa, (u32x2){E.pk[2 * U], E.pk[2 * U + 1]});
}
template <int I>
__device__ __forceinline__ void k4_dma(const K4Ctx& c, int f) {
    const char* src = c.wsrc + (size_t)f * K4_SLOT + c.wid * 4096;
    if constexpr (I == 0)
        asm volatile("s_mov_b32 m0, %2\n\ts_nop 0\n\tglobal_load_lds_dwordx4 %0, %1 sc1" :: "v"(c.voff), "s"(src), "s"(c.ldsbase + (f & 3) * K4_SLOT + c.wid * 4096) : "memory");
    else if constexpr (I < 4)
        asm volatile("global_load_lds_dwordx4 %0, %1 offset:%2 sc1" :: "v"(c.voff), "s"(src), "i"(I * 1024) : "memory");
    else if (c.wid < 4)
        dma1k_dev(c.voff, c.wsrc + (size_t)f * K4_SLOT + (32 + c.wid) * 1024, c.ldsbase + (f & 3) * K4_SLOT + (32 + c.wid) * 1024);
}
template <int N>
__device__ __forceinline__ void k4_xload(u32x4 (&xf)[36], const K4Ctx& c) {
    asm volatile("global_load_dwordx4 %0, %1, %2 offset:%3 sc1" : "=v"(xf[N]) : "v"(c.voffx), "s"(c.hsrc + (N / 4) * 4096), "i"(((N >> 1) & 1) * 2048 + (N & 1) * 256) : "memory");
}
constexpr int k4_xissued(int s) {
    if (s < 0) return 0;
    int a = k4_xissued(s - 1) + 6, b = 2 * (s + 1) + K4_XD;
    a = a < b ? a : b;
    return a < 36 ? a : 36;
}
template <int A, int B>
__device__ __forceinline__ void k4_xloads(u32x4 (&xf)[36], const K4Ctx& c) {
    if constexpr (A < B) { k4_xload<A>(xf, c); k4_xloads<A + 1, B>(xf, c); }
}
template <int S, class ST>
__device__ __forceinline__ void k4_first(K4Acc& acc0, K4Acc& acc1, K4Acc& acc2, u32x4 (&a)[6], u32x4 (&xf)[36], const K4Ctx& c, ST&& stats) {
    if constexpr (S < 18) {
        constexpr int i0 = k4_xissued(S - 1), i1 = k4_xissued(S);
        k4_xloads<i0, i1>(xf, c);
        if constexpr (S == 0) stats();
        asm volatile("s_waitcnt vmcnt(%2)" : "+v"(xf[2 * S]), "+v"(xf[2 * S + 1]) : "i"(i1 - (2 * S + 2)) : "memory");
        auto pair = [&](f32x4& d0, f32x4& d1, auto idx) {
            constexpr int i = decltype(idx)::value;
            d0 = mfma16(a[i], xf[2 * S], S == 0 ? (f32x4){} : d0); PIN(d0); SBAR();
            d1 = mfma16(a[i], xf[2 * S + 1], S == 0 ? (f32x4){} : d1); PIN(d1);
            if constexpr (S < 17) a[i] = lds128(c.slot[i >> 1] + (2 * (S + 1) + (i & 1)) * 1024);
            SBAR();
        };
        pair(acc0.g[0], acc0.g[1], std::integral_constant<int, 0>{});
        pair(acc0.u[0], acc0.u[1], std::integral_constant<int, 1>{});
        if constexpr (S % 2 == 1 && S / 2 < 5) k4_dma<S / 2>(c, 3);
        pair(acc1.g[0], acc1.g[1], std::integral_constant<int, 2>{});
        pair(acc1.u[0], acc1.u[1], std::integral_constant<int, 3>{});
        pair(acc2.g[0], acc2.g[1], std::integral_constant<int, 4>{});
        pair(acc2.u[0], acc2.u[1], std::integral_constant<int, 5>{});
        k4_first<S + 1>(acc0, acc1, acc2, a, xf, c, stats);
    }
}
template <int F, int S, class H>
__device__ __forceinline__ void k4_steps(K4Acc& acc, const K4Acc& accP, const K4Acc& accQ, u32x4 (&a)[K4_NB], u32x4 (&xf)[36], K4Epi& E, const K4Ctx& c, H&& hook) {
    if constexpr (S < 18) {
        constexpr int ig = (2 * S) % K4_NB, iu = (2 * S + 1) % K4_NB;
        auto refill = [&](auto idx, auto frag) {
            constexpr int i = decltype(idx)::value, n = decltype(frag)::value;
            if constexpr (n < 36) a[i] = lds128(c.slot[F & 3] + n * 1024);
            else if constexpr (F >= 4 && F < 8) a[i] = lds128(c.slot[(F + 1) & 3] + (n - 36) * 1024);
        };
        auto epi = [&](auto slot) {
            constexpr int M = decltype(slot)::value;
            if constexpr (F == 3) {
                if constexpr (M == 0) k4_epi_begin(E, c, 1);
                if constexpr (M <= 43) k4_epi_slot<M, 2, 3, 2, 0>(E, accP, c);
                if constexpr (M == 26) k4_epi_begin(E, c, 2);
                if constexpr (M >= 30) k4_epi_slot<M, 30, 3, 2, 0>(E, accQ, c);
                if constexpr (M == 33) k4_store<0>(E, c, 1);
                if constexpr (M == 44) k4_store<1>(E, c, 1);
                if constexpr (M == 61) k4_store<0>(E, c, 2);
                if constexpr (M == 71) k4_store<1>(E, c, 2);
            } else {
                if constexpr (M == 0) k4_epi_begin(E, c, F - 1);
                k4_epi_slot<M, 6, 6, 2, 0>(E, accP, c);
                if constexpr (M == 51) k4_store<0>(E, c, F - 1);
                if constexpr (M == 71) k4_store<1>(E, c, F - 1);
            }
        };
        asm volatile("" :: "v"(a[ig]), "v"(a[iu]));
        acc.g[0] = mfma16(a[ig], xf[2 * S], S == 0 ? (f32x4){} : acc.g[0]); PIN(acc.g[0]);
        epi(std::integral_constant<int, 4 * S>{});
        SBAR();
        acc.g[1] = mfma16(a[ig], xf[2 * S + 1], S == 0 ? (f32x4){} : acc.g[1]); PIN(acc.g[1]);
        refill(std::integral_constant<int, ig>{}, std::integral_constant<int, 2 * S + K4_NB>{});
        epi(std::integral_constant<int, 4 * S + 1>{});
        SBAR();
        acc.u[0] = mfma16(a[iu], xf[2 * S], S == 0 ? (f32x4){} : acc.u[0]); PIN(acc.u[0]);
        hook(IC<S>{});
        if constexpr (F == 3 || F == 4) { if constexpr (S < 10) k4_dma<S % 5>(c, 2 * F - 2 + S / 5); }
        else if constexpr (F == 5) { if constexpr (S % 2 == 0 && S / 2 < 5) k4_dma<S / 2>(c, 8); }
        epi(std::integral_constant<int, 4 * S + 2>{});
        SBAR();
        acc.u[1] = mfma16(a[iu], xf[2 * S + 1], S == 0 ? (f32x4){} : acc.u[1]); PIN(acc.u[1]);
        refill(std::integral_constant<int, iu>{}, std::integral_constant<int, 2 * S + 1 + K4_NB>{});
        epi(std::integral_constant<int, 4 * S + 3>{});
        SBAR();
        k4_steps<F, S + 1>(acc, accP, accQ, a, xf, E, c, hook);
    }
}
template <int M>
__device__ __forceinline__ void k4_epi_all(K4Epi& E, const K4Acc& acc, const K4Ctx& c) {
    if constexpr (M < 40) { k4_epi_slot<M, 0, 3, 1, 0>(E, acc, c); k4_epi_all<M + 1>(E, acc, c); }
}

constexpr int K5_STEP = 20 * 1024, K5_NS = 8;
template <int N>
__device__ __forceinline__ void k5_steps(f32x4 (&acc)[4][6], const u32x4 (&B)[4], const u32x4 (&A)[6], u32x4 (&Bn)[4], u32x4 (&An)[6],
                                         lptr bp, lptr ap) {
    if constexpr (N < 24) {
        constexpr int i = N / 6, jx = N % 6;
        acc[i][jx] = mfma16(A[jx], B[i], acc[i][jx]); PIN(acc[i][jx]);
        if constexpr ((N & 1) == 0 && N / 2 < 10) {
            constexpr int r = N / 2;
            if constexpr (r == 0) Bn[0] = lds128(bp);
            else if constexpr (r <= 6) An[r - 1] = lds128(ap + (r - 1) * 1024);
            else Bn[r - 6] = lds128(bp + ((r - 6) >> 1) * 2048 + ((r - 6) & 1) * 256);
        }
        SBAR();
        k5_steps<N + 1>(acc, B, A, Bn, An, bp, ap);
    }
}

struct K5Load { unsigned cur, n1, n2; int k0, ks; unsigned voff, adst, wdst; const char *asrc, *wsrc; };
__device__ __forceinline__ void k5_issue_a(K5Load& d, int slot) {
    if (d.cur == 0u) { d.cur = d.n1; d.n1 = d.n2; d.n2 = 0u; }
    if (d.cur == 0u) { d.cur = d.n1; d.n1 = 0u; }
    d.ks = d.k0 + __builtin_ctz(d.cur);
    d.cur &= d.cur - 1u;
    asm volatile("s_mov_b32 m0, %2\n\ts_nop 0\n\tglobal_load_lds_dwordx4 %0, %1 sc1\n\tglobal_load_lds_dwordx4 %0, %1 offset:1024 sc1"
                 :: "v"(d.voff), "s"(d.asrc + (size_t)d.ks * 2048), "s"(d.adst + slot * K5_STEP) : "memory");
}
__device__ __forceinline__ void k5_issue_w(const K5Load& d, int slot) {
    asm volatile("s_mov_b32 m0, %2\n\ts_nop 0\n\tglobal_load_lds_dwordx4 %0, %1 sc1\n\tglobal_load_lds_dwordx4 %0, %1 offset:1024 sc1\n\tglobal_load_lds_dwordx4 %0, %1 offset:2048 sc1"
                 :: "v"(d.voff), "s"(d.wsrc + (size_t)d.ks * 12288), "s"(d.wdst + slot * K5_STEP) : "memory");
}
struct K5Poll {
    const char* src; unsigned v, dst; bool on;
    template <class T> __device__ __forceinline__ void operator()(T) const {
        if constexpr (T::value == 16) { if (on) asm volatile("s_mov_b32 m0, %2\n\ts_nop 0\n\tglobal_load_lds_dword %0, %1 sc1" :: "v"(v), "s"(src), "s"(dst) : "memory"); }
    }
};
struct K5Pre {
    K5Load& ld; const K5Poll& poll; bool on;
    template <class T> __device__ __forceinline__ void operator()(T t) const {
        constexpr int S = T::value;
        if constexpr (S >= 2 && S <= 15 && S % 3 == 2) { if (on) k5_issue_a(ld, S / 3 + 2); }
        if constexpr (S >= 2 && S <= 15 && S % 3 == 0) { if (on) k5_issue_w(ld, S / 3 + 1); }
        poll(t);
    }
};

constexpr int KB_OT = 3 * K4_SLOT, KB_RED = K4_CS + 1152, KB_CNT = KVHEAD, KB_STAT = KVHEAD + 64, KB_LDS = K5_NS * K5_STEP;
static_assert(KB_RED + 5 * 32 * 8 <= KB_CNT && KB_STAT + 8192 <= KB_LDS && KB_STAT >= 7 * K5_STEP && KB_LDS <= 163840, "");
static_assert(2 * K5_STEP >= K4_SLOT && 7 * K5_STEP <= K4_CS, "slots 2..6 of phase D lie inside weight slots 1..3 of phase C");

__global__ __launch_bounds__(512, 2) void kb_fused(const u16* __restrict__ x, const u16* __restrict__ wg, const u16* __restrict__ wu,
                                                   const u16* __restrict__ wd, u16* __restrict__ out, char* __restrict__ ws) {
    __shared__ __attribute__((aligned(16))) char smem[KB_LDS];
    lptr lds = (lptr)smem;
    const int bx = blockIdx.x, xcd = bx & 7, j = bx >> 3;
    const int member = 4 * xcd + (j >> 3), qb = j & 7;
    const int wid = __builtin_amdgcn_readfirstlane(threadIdx.x >> 6);
    const unsigned ldsbase = (unsigned)(uintptr_t)lds;
    const int cgB = member & 3, nT = cgB < 2 ? 5 : 4, dTB = (cgB < 2 ? 5 * cgB : 10 + 4 * (cgB - 2)) + (wid < nT ? wid : 0);
    u32x4 af[36];
    {
    const int lane = threadIdx.x & 63;
    const int h = member, tt = 8 * qb + wid;
    const int hh = lane >> 5, l31 = lane & 31;
    const unsigned voff = lane * 16;
    const char* kvsrc = ws + OFF_KV + (size_t)h * KVHEAD;
    {
        const char* s0 = kvsrc + wid * 5120; const unsigned d0 = ldsbase + wid * 5120;
        asm volatile("s_mov_b32 m0, %2\n\ts_nop 0\n\tglobal_load_lds_dwordx4 %0, %1" :: "v"(voff), "s"(s0), "s"(d0) : "memory");
    }
    u32x4 qm; unsigned qt;
    {
        const char* qsrc = ws + OFF_QIMG + (size_t)(h * 64 + tt) * QTILE;
        const unsigned o2 = 1024 + l31 * 4;
        asm volatile("global_load_dwordx4 %0, %2, %4\n\tglobal_load_dword %1, %3, %4" : "=&v"(qm), "=&v"(qt) : "v"(voff), "v"(o2), "s"(qsrc) : "memory");
    }
    {
        const char* s0 = kvsrc + wid * 5120; const unsigned d0 = ldsbase + wid * 5120;
        asm volatile("global_load_lds_dwordx4 %0, %1 offset:1024\n\tglobal_load_lds_dwordx4 %0, %1 offset:2048\n\tglobal_load_lds_dwordx4 %0, %1 offset:3072"
                     :: "v"(voff), "s"(s0) : "memory");
        dma16(voff, s0 + 4096, d0 + 4096);
    }
    lptr kp = lds + lane * 16, ktp = lds + KVTAIL + l31 * 8, vp = lds + 1024 + hh * 304 + (l31 < 18 ? l31 : 18) * 16;
    const bool young = wid >= 4;
#define PRIO(p) do { if (young) __builtin_amdgcn_s_setprio(p); } while (0)
    if (threadIdx.x < 4) *(LAS unsigned*)(lds + KB_CNT + 4 * threadIdx.x) = 0u;
    const SideW sw{wg, wg, wu, wd, ws};
    const int gw = bx * 8 + wid;
    const SideJob job = side_job(sw, gw < 1944 ? gw : 2047, lane);
    asm volatile("s_waitcnt vmcnt(0) lgkmcnt(0)\n\ts_barrier" : "+v"(qm), "+v"(qt) :: "memory");
    PRIO(1);
    const u32x2 qtA = {hh == 0 ? qt : 0u, 0u}, qtB = {0u, hh == 0 ? qt : 0u};
    f32x16 o = {}, sA, sB;
    K2Regs R;
    R.km = lds128(kp); R.kt = lds64(ktp);
    sA = mfma32(R.km, qm, mfma32k8(R.kt, qtA, (f32x16){}));
    R.km = lds128(kp + KVT);
    R.v0 = lds128(vp); R.v1 = lds128(vp + 608);
    R.e0 = __builtin_amdgcn_exp2f(sA[0]); R.e1 = __builtin_amdgcn_exp2f(sA[1]);
#define TWO(HA, HB) do { \
        k2_step<false, true, true, true>(sA, sB, o, R, qm, qtA, qtB, kp, ktp, vp, HA); \
        k2_step<true, true, true, true>(sB, sA, o, R, qm, qtA, qtB, kp, ktp, vp, HB); \
        kp += KVPAIR; ktp += KVPAIR; vp += KVPAIR; } while (0)
    const NoHook nh;
#define TWO_DMA(c) TWO(([&](auto p) { k2_piece<decltype(p)::value>(c, wid, voff, kvsrc, ldsbase); }), ([&](auto p) { k2_piece<2 + decltype(p)::value>(c, wid, voff, kvsrc, ldsbase); }))
    TWO_DMA(0); TWO_DMA(1); TWO_DMA(2); TWO_DMA(3);
    TWO(nh, nh);
    PRIO(0);
    TWO(nh, nh);
    TWO(nh, nh);
    asm volatile("s_waitcnt vmcnt(0)\n\ts_barrier" ::: "memory");
    PRIO(1);
    const lptr sbuf = lds + wid * 4096;
    f32x4 cs[4] = {};
    side_fetch(job, 0, ldsbase + wid * 4096);
    int t = 14;
    constexpr int K2_S1 = 24, K2_S2 = 34, K2_SW = 52;
    for (; t < K2_S1; t += 2) TWO(nh, nh);
    WAIT_VM(0);
    side_finish(job, 0, sbuf, cs, lane);
    asm volatile("s_waitcnt lgkmcnt(0)" ::: "memory");
    side_fetch(job, 1, ldsbase + wid * 4096);
    for (; t < K2_S2; t += 2) TWO(nh, nh);
    asm volatile("s_waitcnt vmcnt(0)" ::: "memory");
    side_finish(job, 1, sbuf, cs, lane);
    for (; t < K2_S2 + 6; t += 2) TWO(nh, nh);
    WAIT_VM(0);
    if (lane == 0) {
        if (__hip_atomic_fetch_add((LAS unsigned*)(lds + KB_CNT), 1u, __ATOMIC_RELAXED, __HIP_MEMORY_SCOPE_WORKGROUP) == 7u) flag_add(ws, FL_PRE + (bx & 15) * 32);
    }
    static_assert(K2_S2 + 6 == 40 && K2_SW == 52, "");
    if (wid < nT) {
        const char* wsrc = ws + OFF_WOT + (size_t)dTB * 36 * 1024;
#define AF2(n) TWO(([&](auto p) { k3_aload<(n) + decltype(p)::value>(af, wsrc, voff); }), ([&](auto p) { k3_aload<(n) + 2 + decltype(p)::value>(af, wsrc, voff); }))
        AF2(0); AF2(4); AF2(8); AF2(12); AF2(16); AF2(20);
        PRIO(0);
        AF2(24); AF2(28); AF2(32);
#undef AF2
        t = 58;
        asm volatile("; with prefetch");
    } else {
#pragma unroll 2
        for (; t < K2_SW; t += 2) TWO(nh, nh);
        PRIO(0);
        for (; t < 58; t += 2) TWO(nh, nh);
        asm volatile("; without prefetch");
    }
    for (; t < 62; t += 2) TWO(nh, nh);
    k2_step<false, true, false, true>(sA, sB, o, R, qm, qtA, qtB, kp, ktp, vp, nh);
    k2_step<true, false, false, false>(sB, sA, o, R, qm, qtA, qtB, kp, ktp, vp, nh);
#undef TWO
#undef TWO_DMA
#undef PRIO
    float l = __shfl(o[10], l31);
    float fin = 0.f;
#pragma unroll
    for (int r = 0; r < 8; ++r) fin = __builtin_fmaf(o[r], 0.f, fin);
    if (hh == 0) fin = __builtin_fmaf(o[8], 0.f, __builtin_fmaf(o[9], 0.f, fin));
    const bool bad = !(l > 8.673617379884035e-19f && l < 1.152921504606847e18f) || fin != 0.f;
    if (__builtin_expect(__any(bad), 0)) {
        o = (f32x16){}; float m = -3.0e38f;
        const char* kq = kvsrc + lane * 16; const char* ktq = kvsrc + KVTAIL + l31 * 8; const char* vq = kvsrc + 1024 + hh * 304 + (l31 < 18 ? l31 : 18) * 16;
        for (int tb = 0; tb < 64; ++tb) {
            const int off = (tb >> 1) * KVPAIR, sub = (tb & 1) * KVT;
            const u32x4 km = *(const u32x4*)(kq + off + sub), v0 = *(const u32x4*)(vq + off + sub), v1 = *(const u32x4*)(vq + off + sub + 608);
            const u32x2 kt = *(const u32x2*)(ktq + off);
            const f32x16 s = mfma32(km, qm, mfma32k8(kt, (tb & 1) ? qtB : qtA, (f32x16){}));
            float mx = s[0];
#pragma unroll
            for (int r = 1; r < 16; ++r) mx = fmaxf(mx, s[r]);
            mx = fmaxf(mx, xhalf(mx));
            const float mn = fmaxf(m, mx), al = __builtin_amdgcn_exp2f(m - mn);
            m = mn;
            unsigned pw[8];
#pragma unroll
            for (int i = 0; i < 8; ++i) pw[i] = pk2(__builtin_amdgcn_exp2f(s[2 * i] - mn), __builtin_amdgcn_exp2f(s[2 * i + 1] - mn));
#pragma unroll
            for (int r = 0; r < 16; ++r) o[r] *= al;
            o = mfma32(v0, (u32x4){pw[0], pw[1], pw[2], pw[3]}, o);
            o = mfma32(v1, (u32x4){pw[4], pw[5], pw[6], pw[7]}, o);
        }
        l = __shfl(o[10], l31);
    }
    const float inv = 1.0f / l;
    char* ot = ws + OFF_OT + (size_t)tt * 36 * 1024;
    st16(ot + h * 1024 + lane * 16, (u32x4){pk2(o[0] * inv, o[1] * inv), pk2(o[2] * inv, o[3] * inv), pk2(o[4] * inv, o[5] * inv), pk2(o[6] * inv, o[7] * inv)});
    if (hh == 0) st4(ot + (32 + (h >> 3)) * 1024 + (((h & 7) >> 2) * 32 + l31) * 16 + (h & 3) * 4, pk2(o[8] * inv, o[9] * inv));
    }
    int tz = threadIdx.x; asm volatile("" : "+v"(tz));
    const int lane = tz & 63;
    const unsigned voff = lane * 16;
    K4Ctx c;
    c.ldsbase = ldsbase; c.wid = wid; c.voff = voff;
    const int mtC = 2 * (xcd & 3) + (j & 1), ftC = 9 * (16 * (xcd >> 2) + (j >> 1));
    c.wsrc = ws + OFF_WGU + (size_t)ftC * K4_SLOT;
    const bool actD = j < 30;
    int sp, m4, dg, k0, fgw;
    {
        const int idxD = xcd * 30 + (actD ? j : 0), remD = idxD % 48;
        sp = idxD / 48; m4 = remD / 3; dg = remD % 3;
        k0 = 29 * sp;
        const int fgA = (2 * k0) / 9, nfg = (2 * (k0 + (sp < 4 ? 29 : 28)) - 1) / 9 - fgA + 1;
        fgw = FL_C + ((m4 >> 1) * 8 + 4 * (m4 & 1) + (wid & 3)) * 32 + fgA + (nfg << 16);
        asm volatile("" : "+s"(sp), "+s"(m4), "+s"(dg), "+s"(k0), "+s"(fgw));
    }
    const int nk = sp < 4 ? 29 : 28, nst = nk / 2, remD = 3 * m4 + dg;
    {
        const int tt = 8 * qb + (member >> 2), cg = cgB;
        const int hh = lane >> 5, l31 = lane & 31;
        LAS f32x2* red = (LAS f32x2*)(lds + KB_RED);
        WAIT_VM(0);
        if (lane == 0) flag_set(ws, FL_A + (qb * 8 + wid) * 32 + member);
        if (wid < nT) {
            const int dT = dTB;
            u32x4 xr0, xr1;
            k3_pin<0>(af);
            asm volatile("s_barrier" ::: "memory");
            {
                const u16* xr = x + (size_t)(32 * tt + l31) * D_ + 32 * dT + 8 * hh;
                asm volatile("global_load_dwordx4 %0, %2, off\n\tglobal_load_dwordx4 %1, %2, off offset:32" : "=&v"(xr0), "=&v"(xr1) : "v"(xr) : "memory");
            }
            lds_wait(lds + KB_CNT + 8);
            u32x4 b[4];
            lptr bp = lds + KB_OT + lane * 16;
#pragma unroll
            for (int k = 0; k < 4; ++k) b[k] = lds128(bp + k * 1024);
            f32x16 acc;
            k3_steps<0>(acc, af, b, bp);
            asm volatile("s_waitcnt vmcnt(0)" : "+v"(xr0), "+v"(xr1) :: "memory");
            float s1 = 0.f, s2 = 0.f;
            unsigned hp[8];
#pragma unroll
            for (int i = 0; i < 8; ++i) {
                const unsigned xv = i < 4 ? xr0[i] : xr1[i - 4];
                hp[i] = pk2(acc[2 * i] + bflo(xv), acc[2 * i + 1] + bfhi(xv));
                const float a = bflo(hp[i]), bb = bfhi(hp[i]);
                s1 += a + bb; s2 += a * a + bb * bb;
            }
            char* ht = ws + OFF_HT + (size_t)(tt * 36 + 2 * dT) * 1024 + lane * 16;
            st16(ht, (u32x4){hp[0], hp[1], hp[2], hp[3]});
            st16(ht + 1024, (u32x4){hp[4], hp[5], hp[6], hp[7]});
            s1 += xhalf(s1); s2 += xhalf(s2);
            if (hh == 0) red[wid * 32 + l31] = (f32x2){s1, s2};
            asm volatile("s_waitcnt lgkmcnt(0)" ::: "memory");
            unsigned arrived = 0;
            if (lane == 0) arrived = __hip_atomic_fetch_add((LAS unsigned*)(lds + KB_CNT + 4), 1u, __ATOMIC_RELAXED, __HIP_MEMORY_SCOPE_WORKGROUP);
            if (__builtin_amdgcn_readfirstlane(arrived) == (unsigned)(nT - 1) && lane < 32) {
                f32x2 r = red[l31];
#pragma unroll
                for (int i = 1; i < 4; ++i) r += red[i * 32 + l31];
                if (nT == 5) r += red[4 * 32 + l31];
                st8(ws + OFF_STAT + (size_t)(cg * 2048 + 32 * tt + l31) * 8, __builtin_bit_cast(u32x2, r));
            }
        } else {
            asm volatile("s_barrier" ::: "memory");
            if (wid == 7) {
                flag_wait(ws, FL_A + (qb * 8 + (tt & 7)) * 32 + (lane & 31), 1u);
                const char* osrc = ws + OFF_OT + (size_t)tt * 36 * 1024;
#pragma unroll
                for (int i = 0; i < 9; ++i) dma4k_dev(voff, osrc + i * 4096, ldsbase + KB_OT + i * 4096);
                if (lane == 0) __hip_atomic_store((LAS unsigned*)(lds + KB_CNT + 12), 1u, __ATOMIC_RELAXED, __HIP_MEMORY_SCOPE_WORKGROUP);
                WAIT_VM(0);
                if (lane == 0) __hip_atomic_store((LAS unsigned*)(lds + KB_CNT + 8), 1u, __ATOMIC_RELAXED, __HIP_MEMORY_SCOPE_WORKGROUP);
            } else if (wid >= 5) {
                flag_wait(ws, FL_PRE + (lane & 15) * 32, 16u);
                const int w5 = wid - 5;
                float cp[3][9];
#pragma unroll
                for (int r = 0; r < 3; ++r) {
                    const int cc = 144 * w5 + 64 * r + lane;
                    const char* p = ws + OFF_CSP + (size_t)ftC * 128;
#pragma unroll
                    for (int i = 0; i < 9; ++i)
                        asm volatile("global_load_dword %0, %1, %2 sc1" : "=v"(cp[r][i]) : "v"((unsigned)(((r < 2 || lane < 16) ? cc : 0) * 4)), "s"(p + (size_t)i * 288 * 128) : "memory");
                }
                lds_wait(lds + KB_CNT + 12);
#pragma unroll
                for (int i = 0; i < 13; ++i) dma4k_dev(voff, c.wsrc + (2 * i + w5) * 4096, ldsbase + (2 * i + w5) * 4096);
                if (w5 == 0) dma4k_dev(voff, c.wsrc + 26 * 4096, ldsbase + 26 * 4096);
                asm volatile("s_waitcnt vmcnt(52)" : "+v"(cp[0][0]), "+v"(cp[0][1]), "+v"(cp[0][2]), "+v"(cp[0][3]), "+v"(cp[0][4]), "+v"(cp[0][5]), "+v"(cp[0][6]), "+v"(cp[0][7]), "+v"(cp[0][8]) :: "memory");
                asm volatile("" : "+v"(cp[1][0]), "+v"(cp[1][1]), "+v"(cp[1][2]), "+v"(cp[1][3]), "+v"(cp[1][4]), "+v"(cp[1][5]), "+v"(cp[1][6]), "+v"(cp[1][7]), "+v"(cp[1][8]));
                asm volatile("" : "+v"(cp[2][0]), "+v"(cp[2][1]), "+v"(cp[2][2]), "+v"(cp[2][3]), "+v"(cp[2][4]), "+v"(cp[2][5]), "+v"(cp[2][6]), "+v"(cp[2][7]), "+v"(cp[2][8]));
                LAS float* csl = (LAS float*)(lds + K4_CS) + 144 * w5;
                float s0 = 0.f, s1 = 0.f, s2 = 0.f;
#pragma unroll
                for (int i = 0; i < 9; ++i) { s0 += cp[0][i]; s1 += cp[1][i]; s2 += cp[2][i]; }
                csl[lane] = s0; csl[64 + lane] = s1;
                if (lane < 16) csl[128 + lane] = s2;
            }
        }
        asm volatile("s_waitcnt vmcnt(0) lgkmcnt(0)\n\ts_barrier" ::: "memory");
        if (wid == 7) {
            if (lane == 0) flag_set(ws, FL_B + qb * 32 + member);
            flag_wait(ws, FL_B + mtC * 32 + (lane & 31), 1u);
        }
        asm volatile("s_barrier" ::: "memory");
    }
    const int tt = 8 * mtC + wid;
    const int l15 = lane & 15, kq = lane >> 4;
    c.voffx = (kq >> 1) * 1024 + (kq & 1) * 512 + l15 * 16;
    c.voffa = (kq >> 1) * 512 + l15 * 16 + (kq & 1) * 8;
    c.hsrc = ws + OFF_HT + (size_t)tt * 36 * 1024;
    u32x4 xf[36];
    auto stats = [&]() { dma1k_dev(voff, ws + OFF_STAT + (size_t)((wid >> 1) * 2048 + 256 * mtC + 128 * (wid & 1)) * 8, ldsbase + KB_STAT + wid * 1024); };
    c.adst = ws + OFF_ACT + ((size_t)tt * 288 + ftC) * 1024;
    c.csw = (LAS const float*)(lds + K4_CS) + 4 * kq;
#pragma unroll
    for (int i = 0; i < 4; ++i) { c.slot[i] = lds + i * K4_SLOT + lane * 16; PIN(c.slot[i]); }
    K4Acc accA, accB, accC; K4Epi E;
#define K4_BAR(N) asm volatile("s_waitcnt vmcnt(" #N ")\n\ts_barrier" ::: "memory")
    {
        u32x4 a6[6];
#pragma unroll
        for (int i = 0; i < 6; ++i) a6[i] = lds128(c.slot[i >> 1] + (i & 1) * 1024);
        k4_first<0>(accA, accB, accC, a6, xf, c, stats);
    }
    K4_BAR(0);
    u32x4 a[K4_NB];
#define K4_PRIME(F) _Pragma("unroll") for (int k = 0; k < K4_NB; ++k) a[k] = lds128(c.slot[(F) & 3] + k * 1024)
    K4_PRIME(3);
#pragma unroll
    for (int u = 0; u < 2; ++u) {
        LAS const f32x2* sp = (LAS const f32x2*)(lds + KB_STAT) + 32 * wid + 16 * u + l15;
        const f32x2 st = sp[0] + sp[256] + sp[512] + sp[768];
        c.m[u] = st[0] * (1.f / 576.f);
        c.V[u] = fmaxf(st[1] * (1.f / 576.f) - c.m[u] * c.m[u], 0.f) + 1e-5f;
        const float rstd = __builtin_amdgcn_rsqf(c.V[u]);
        c.C0[u] = -2.f * 0.7978845608028654f * 1.4426950408889634f * rstd; c.C1[u] = c.C0[u] * 0.044715f * rstd * rstd;
    }
    k4_epi_begin(E, c, 0);
    k4_epi_all<0>(E, accA, c);
    k4_store<0>(E, c, 0); k4_store<1>(E, c, 0);
    SBAR();
    const NoHook nohook;
    k4_steps<3, 0>(accA, accB, accC, a, xf, E, c, nohook);
    K4_BAR(3); K4_PRIME(4);
    k4_steps<4, 0>(accB, accA, accA, a, xf, E, c, nohook);
    K4_BAR(6);
    k4_steps<5, 0>(accA, accB, accB, a, xf, E, c, nohook);
    K4_BAR(6);
    k4_steps<6, 0>(accB, accA, accA, a, xf, E, c, nohook);
    K4_BAR(4);
    const int flagC = FL_C + (mtC * 8 + wid) * 32 + 16 * (xcd >> 2) + (j >> 1);
    WAIT_VM(2);
    if (lane == 0) flag_set(ws, flagC, 1u);
    const int l = wid & 3;
    const int flagD = (fgw & 0xffff) + ((lane & 7) < (fgw >> 16) ? (lane & 7) : 0);
    K5Load ld;
    {
        constexpr unsigned long long patA = 0x63ull | (0x63ull << 9) | (0x63ull << 18) | (0x63ull << 27) | (0x63ull << 36) | (0x63ull << 45);
        constexpr unsigned long long patB = 0x04ull | (0x04ull << 9) | (0x04ull << 18) | (0x04ull << 27) | (0x04ull << 36) | (0x04ull << 45);
        const unsigned all = (1u << nk) - 1u;
        ld.cur = (unsigned)(patA >> (k0 % 9)) & all; ld.n1 = (unsigned)(patB >> (k0 % 9)) & all; ld.n2 = all & ~(ld.cur | ld.n1);
        ld.k0 = k0; ld.voff = voff;
        ld.asrc = ws + OFF_ACT + (size_t)(4 * m4 + l) * 288 * 1024;
        ld.wsrc = ws + OFF_WDT + ((size_t)dg * 144 * 12 + 3 * l) * 1024;
        ld.adst = ldsbase + l * 2048; ld.wdst = ldsbase + 8192 + l * 3072;
    }
    const int pL = nk - __builtin_popcount(ld.n2);
    const bool helper = actD && wid < 4;
    K5Poll poll{ws + OFF_FLAG, (unsigned)(flagD * 4), ldsbase + KB_STAT + l * 256, helper};
    k4_steps<7, 0>(accA, accB, accB, a, xf, E, c, poll);
    if (wid < 4) WAIT_VM(1); else WAIT_VM(2);
    const bool pre = helper && __all(*(LAS const unsigned*)(lds + KB_STAT + l * 256 + lane * 4) >= 1u);
    asm volatile("s_waitcnt lgkmcnt(0)\n\ts_barrier ; E_pre" ::: "memory");
    if (lane == 0) flag_set(ws, flagC, 2u);
    poll.dst += 1024;
    K5Pre pref{ld, poll, pre};
    k4_steps<8, 0>(accB, accA, accA, a, xf, E, c, pref);
    k4_epi_begin(E, c, 8);
    k4_epi_all<0>(E, accB, c);
    k4_store<0>(E, c, 8); k4_store<1>(E, c, 8);
#undef K4_BAR
#undef K4_PRIME
    if (!actD) {
        WAIT_VM(0);
        if (lane == 0) flag_set(ws, flagC, 3u);
        return;
    }
    if (wid >= 4) {
        asm volatile("s_barrier ; E0" ::: "memory");
        const unsigned did = *(LAS const unsigned*)(lds + KB_STAT + 2048 + l * 4);
        if (!(did & 2u)) flag_wait(ws, flagD, 2u);
        if (did & 1u) {
#pragma unroll
            for (int p = 0; p < 5; ++p) ld.cur &= ld.cur - 1u;
        } else {
#pragma unroll 1
            for (int p = 0; p < 5; ++p) { k5_issue_a(ld, (p + 2) & 7); k5_issue_w(ld, (p + 2) & 7); }
            WAIT_VM(15);
        }
        asm volatile("s_barrier ; #0" ::: "memory");
        int p = 5;
        auto issue2 = [&]() {
            if (p < nk) { k5_issue_a(ld, (p + 2) & 7); k5_issue_w(ld, (p + 2) & 7); }
            if (p + 1 < nk) { k5_issue_a(ld, (p + 3) & 7); k5_issue_w(ld, (p + 3) & 7); }
            p += 2;
        };
        issue2();
        WAIT_VM(10);
        if (lane == 0) flag_set(ws, flagC, 3u);
        asm volatile("s_barrier ; #1" ::: "memory");
        issue2();
        const int bL = (pL - 5) >> 1;
        int b = 2;
        for (; b < bL - 1; ++b) { WAIT_VM(15); __builtin_amdgcn_s_barrier(); issue2(); }
        {
            unsigned prog;
            WAIT_VM(15);
            asm volatile("s_barrier ; #(bL - 1)" ::: "memory");
            asm volatile("global_load_dword %0, %1, %2 sc1" : "=v"(prog) : "v"((unsigned)(flagD * 4)), "s"(ws + OFF_FLAG) : "memory");
            issue2();
            asm volatile("s_waitcnt vmcnt(10)\n\ts_barrier ; #bL" : "+v"(prog) :: "memory");
            if (!__all(prog >= 3u)) flag_wait(ws, flagD, 3u);
            issue2();
            b = bL + 1;
        }
        for (; b <= nst; ++b) {
            const int fly = (p < nk ? p : nk) - (2 * b + 2);
            if (fly >= 3) WAIT_VM(15); else if (fly == 2) WAIT_VM(10); else if (fly == 1) WAIT_VM(5); else WAIT_VM(0);
            __builtin_amdgcn_s_barrier();
            issue2();
        }
        return;
    }
    {
        WAIT_VM(3);
        const bool okB = __all(*(LAS const unsigned*)(lds + KB_STAT + 1024 + l * 256 + lane * 4) >= 2u);
        if (lane == 0) *(LAS unsigned*)(lds + KB_STAT + 2048 + l * 4) = (pre ? 1u : 0u) | (okB ? 2u : 0u);
        asm volatile("s_waitcnt lgkmcnt(0)\n\ts_barrier ; E0 " ::: "memory");
        WAIT_VM(0);
        if (lane == 0) flag_set(ws, flagC, 3u);
        const int a = wid >> 1, b = wid & 1;
        int tzd = threadIdx.x; asm volatile("" : "+v"(tzd));
        const int laned = tzd & 63;
        lptr bp0 = lds + (2 * a) * 2048 + (laned >> 5) * 1024 + ((laned >> 4) & 1) * 512 + (laned & 15) * 16, ap0 = lds + 8192 + (6 * b) * 1024 + laned * 16;
        f32x4 acc[4][6] = {};
        u32x4 B0[4], A0[6], B1[4], A1[6];
        asm volatile("s_barrier ; #0" ::: "memory");
#pragma unroll
        for (int i = 0; i < 4; ++i) B0[i] = lds128(bp0 + 2 * K5_STEP + (i >> 1) * 2048 + (i & 1) * 256);
#pragma unroll
        for (int i = 0; i < 6; ++i) A0[i] = lds128(ap0 + 2 * K5_STEP + i * 1024);
        int slot = 2;
        for (int s = 0; s < nst; ++s) {
            const int s1 = (slot + 1) & 7, s2 = (slot + 2) & 7;
            k5_steps<0>(acc, B0, A0, B1, A1, bp0 + s1 * K5_STEP, ap0 + s1 * K5_STEP);
            asm volatile("s_barrier" ::: "memory");
            k5_steps<0>(acc, B1, A1, B0, A0, bp0 + s2 * K5_STEP, ap0 + s2 * K5_STEP);
            slot = s2;
        }
        if (nk & 1) k5_steps<0>(acc, B0, A0, B1, A1, bp0, ap0);
        char* pbase = ws + OFF_PART + (size_t)remD * (25 * 12 * 1024) + wid * (3 * 1024) + laned * 16;
        const int l15d = laned & 15, kqd = laned >> 4;
#pragma unroll
        for (int n = 0; n < 12; ++n) {
            const int i = n / 3, p = n % 3, own = n % 5;
            if (own != sp) {
                const f32x4 v0 = acc[i][2 * p], v1 = acc[i][2 * p + 1];
                st16(pbase + (size_t)(own * 5 + sp) * (12 * 1024) + (n / 5) * 1024, (u32x4){pk2(v0[0], v0[1]), pk2(v0[2], v0[3]), pk2(v1[0], v1[1]), pk2(v1[2], v1[3])});
            }
        }
        u32x4 hres[3];
#pragma unroll
        for (int sl = 0; sl < 3; ++sl) {
            const int n = 5 * sl + sp < 12 ? 5 * sl + sp : sp, i = n / 3, p = n % 3;
            const int tt = 4 * m4 + 2 * a + (i >> 1), dT = 6 * dg + 3 * b + p;
            const char* hp = ws + OFF_HT + (size_t)(tt * 36 + 2 * dT + (kqd >> 1)) * 1024 + (kqd & 1) * 512 + (16 * (i & 1) + l15d) * 16;
            asm volatile("global_load_dwordx4 %0, %1, off sc1" : "=v"(hres[sl]) : "v"(hp) : "memory");
        }
        asm volatile("s_waitcnt vmcnt(0)" : "+v"(hres[0]), "+v"(hres[1]), "+v"(hres[2]) :: "memory");
        if (laned == 0) flag_set(ws, FL_D + (remD * 4 + wid) * 8 + sp);
        flag_wait(ws, FL_D + (remD * 4 + wid) * 8 + (laned < 5 ? laned : 0), 1u);
        u32x4 q[3][4];
#pragma unroll
        for (int sl = 0; sl < 3; ++sl) {
            const int sl_ok = (5 * sl + sp < 12) ? sl : 0;
#pragma unroll
            for (int k = 0; k < 4; ++k) {
                const int srcs = k < sp ? k : k + 1;
                asm volatile("global_load_dwordx4 %0, %1, off sc1" : "=v"(q[sl][k]) : "v"(pbase + (size_t)(sp * 5 + srcs) * (12 * 1024) + sl_ok * 1024) : "memory");
            }
        }
        asm volatile("s_waitcnt vmcnt(0)" : "+v"(q[0][0]), "+v"(q[0][1]), "+v"(q[0][2]), "+v"(q[0][3]), "+v"(q[1][0]), "+v"(q[1][1]), "+v"(q[1][2]), "+v"(q[1][3]),
                     "+v"(q[2][0]), "+v"(q[2][1]), "+v"(q[2][2]), "+v"(q[2][3]) :: "memory");
#pragma unroll
        for (int n = 0; n < 12; ++n) {
            const int i = n / 3, p = n % 3, own = n % 5;
            if (own == sp) {
                f32x4 v0 = acc[i][2 * p], v1 = acc[i][2 * p + 1];
#pragma unroll
                for (int k = 0; k < 4; ++k) {
                    const u32x4 w = q[n / 5][k];
                    v0 += (f32x4){bflo(w[0]), bfhi(w[0]), bflo(w[1]), bfhi(w[1])}; v1 += (f32x4){bflo(w[2]), bfhi(w[2]), bflo(w[3]), bfhi(w[3])};
                }
                const u32x4 h = hres[n / 5];
                const u32x4 o = {pk2(v0[0] + bflo(h[0]), v0[1] + bfhi(h[0])), pk2(v0[2] + bflo(h[1]), v0[3] + bfhi(h[1])),
                                 pk2(v1[0] + bflo(h[2]), v1[1] + bfhi(h[2])), pk2(v1[2] + bflo(h[3]), v1[3] + bfhi(h[3]))};
                const int tt = 4 * m4 + 2 * a + (i >> 1), dT = 6 * dg + 3 * b + p;
                *(u32x4*)(out + (size_t)(32 * tt + 16 * (i & 1) + l15d) * D_ + 32 * dT + 8 * kqd) = o;
            }
        }
    }
}

extern "C" void kernel_launch(void* const* d_in, const int* in_sizes, int n_in, void* d_out, int out_size, void* d_ws, size_t ws_size,
                              hipStream_t stream) {
    const u16* x = (const u16*)d_in[0];
    const u16* wqkv = (const u16*)d_in[1];
    const u16* wo = (const u16*)d_in[2];
    const u16* wg = (const u16*)d_in[4];
    const u16* wu = (const u16*)d_in[5];
    const u16* wd = (const u16*)d_in[6];
    char* ws = (char*)d_ws;
    k1_qkv<<<256, 512, 0, stream>>>(x, wqkv, wo, wg, wu, wd, (const u16*)d_out, ws);
    kb_fused<<<256, 512, 0, stream>>>(x, wg, wu, wd, (u16*)d_out, ws);
}
```
